```python
import torch

def _softmax(x, axis=-1):
    return torch.softmax(x, dim=axis)

def _gelu(x):
    return torch.nn.functional.gelu(x, approximate='tanh')

def _silu(x):
    return torch.nn.functional.silu(x)

def _sigmoid(x):
    return torch.sigmoid(x)

def _layernorm(x, axis=-1, eps=1e-05):
    m = x.mean(dim=axis, keepdim=True)
    v = x.var(dim=axis, keepdim=True, unbiased=False)
    return (x - m) / torch.sqrt(v + eps)

def _scatter_add(tgt, idx, src, axis=0):
    return tgt.clone().index_add_(axis, idx, src)

def _segment_sum(src, seg, n):
    out = torch.zeros((n,) + src.shape[1:], dtype=src.dtype)
    return out.index_add_(0, seg, src)

def _pad(x, pw):
    flat = tuple((v for pair in reversed(pw) for v in pair))
    return torch.nn.functional.pad(x, flat)
kernel_name = 'sequential_preLN__memory_aug_recurrent_mem_tr__glu_gated_bilinear__prompt'

def setup_inputs():

    _w = lambda a: (lambda t: t.to(torch.bfloat16) if t.is_floating_point() else t)(torch.as_tensor(a))

    def _A_setup():
        B, S, Dm = (1, 2048, 512)
        H, Dh = (32, 128)
        M, seg = (8, 32)
        seg = min(seg, S)
        S = S // seg * seg
        return {'x': _w((lambda *s: torch.randn(*s, dtype=torch.bfloat16) * 0.05)(B, S, Dm)), 'mem0': _w((lambda *s: torch.randn(*s, dtype=torch.bfloat16) * 0.2)(B, M, Dm)), 'Wq': _w((lambda *s: torch.randn(*s, dtype=torch.bfloat16) * 1.6)(Dm, H, Dh) * Dm ** (-0.5)), 'Wk': _w((lambda *s: torch.randn(*s, dtype=torch.bfloat16) * 1.6)(Dm, H, Dh) * Dm ** (-0.5)), 'Wv': _w((lambda *s: torch.randn(*s, dtype=torch.bfloat16) * 0.2)(Dm, H, Dh) * Dm ** (-0.5)), 'Wo': _w((lambda *s: torch.randn(*s, dtype=torch.bfloat16) * 0.2)(H, Dh, Dm) * (H * Dh) ** (-0.5)), 'seg_len': seg}
    _A_in = _A_setup()

    def _B_setup():
        B, S, D, F = (1, 2048, 512, 1792)
        return {'x': _w((lambda *s: torch.randn(*s, dtype=torch.bfloat16) * 0.05)(B, S, D)), 'w_gate': _w((lambda *s: torch.randn(*s, dtype=torch.bfloat16) * 0.2)(D, F) * D ** (-0.5)), 'w_up': _w((lambda *s: torch.randn(*s, dtype=torch.bfloat16) * 0.2)(D, F) * D ** (-0.5)), 'w_down': _w((lambda *s: torch.randn(*s, dtype=torch.bfloat16) * 0.2)(F, D) * F ** (-0.5))}
    _B_in = _B_setup()
    return {'A_x': _A_in['x'], 'A_mem0': _A_in['mem0'], 'A_Wq': _A_in['Wq'], 'A_Wk': _A_in['Wk'], 'A_Wv': _A_in['Wv'], 'A_Wo': _A_in['Wo'], 'A_seg_len': _A_in['seg_len'], 'B_x': _B_in['x'], 'B_w_gate': _B_in['w_gate'], 'B_w_up': _B_in['w_up'], 'B_w_down': _B_in['w_down']}

def reference(A_x, A_mem0, A_Wq, A_Wk, A_Wv, A_Wo, A_seg_len, B_x, B_w_gate, B_w_up, B_w_down):

    def _A_ref(x, mem0, Wq, Wk, Wv, Wo, seg_len):
        B, S, Dm = x.shape
        H, Dh = (Wq.shape[1], Wq.shape[2])
        M = mem0.shape[1]
        scale = Dh ** (-0.5)
        n_seg = S // seg_len
        L = M + seg_len
        idx = torch.arange(L)
        mask = (idx[:, None] < M) | (idx[:, None] >= idx[None, :])
        mem = mem0
        outs = []
        for s in range(n_seg):
            xs = x[:, s * seg_len:(s + 1) * seg_len, :].clone()
            h = torch.concatenate([mem, xs], axis=1)
            q = torch.einsum('btd,dhe->bhte', h, Wq)
            k = torch.einsum('btd,dhe->bhte', h, Wk)
            v = torch.einsum('btd,dhe->bhte', h, Wv)
            scores = torch.einsum('bhqe,bhke->bhqk', q, k) * scale
            scores = torch.where(mask[None, None], scores, -1000000000.0)
            p = _softmax(scores, axis=-1)
            o = torch.einsum('bhqk,bhke->bhqe', p, v)
            o = torch.einsum('bhte,hed->btd', o, Wo)
            mem = o[:, :M, :].clone()
            outs.append(o[:, M:, :])
        return torch.concatenate(outs, axis=1)

    def _B_ref(x, w_gate, w_up, w_down):
        g = torch.einsum('bsd,df->bsf', x, w_gate)
        u = torch.einsum('bsd,df->bsf', x, w_up)
        h = g * u
        return torch.einsum('bsf,fd->bsd', h, w_down)
    x = A_x
    h = x + _A_ref(_layernorm(x, axis=-1), A_mem0, A_Wq, A_Wk, A_Wv, A_Wo, A_seg_len)
    h2 = h + _B_ref(_layernorm(h, axis=-1), B_w_gate, B_w_up, B_w_down)
    return h2
```

```cpp
#include <hip/hip_runtime.h>
#include <hip/hip_bf16.h>

typedef unsigned short u16;
typedef __bf16 bf16x8 __attribute__((ext_vector_type(8)));
typedef __bf16 bf16x16 __attribute__((ext_vector_type(16)));
typedef float f32x4 __attribute__((ext_vector_type(4)));
typedef unsigned v4u __attribute__((ext_vector_type(4)));
typedef unsigned v2u __attribute__((ext_vector_type(2)));
typedef short v4s __attribute__((ext_vector_type(4)));
#define LAS __attribute__((address_space(3)))

constexpr int S_ = 2048, DM = 512, NH = 32, DH = 128, HD = NH * DH, MEMN = 8, SEG = 32, NSEG = 64, FF = 1792;

__device__ __forceinline__ float bf2f(u16 v) { return __uint_as_float(((unsigned)v) << 16); }
__device__ __forceinline__ u16 f2bf(float f) { return __builtin_bit_cast(u16, (__bf16)f); }
__device__ __forceinline__ float bfr(float f) { return bf2f(f2bf(f)); }
__device__ __forceinline__ unsigned pack2(float a, float b) { return (unsigned)f2bf(a) | ((unsigned)f2bf(b) << 16); }
__device__ __forceinline__ unsigned nosent(unsigned w) { return w == 0xFFFFFFFFu ? 0x7FFF7FFFu : w; }
__device__ __forceinline__ v2u tr_read(const void* p) {
    return __builtin_bit_cast(v2u, __builtin_amdgcn_ds_read_tr16_b64_v4i16((LAS v4s*)p));
}
__device__ __forceinline__ bf16x8 mk8(v2u a, v2u b) { v4u t = {a.x, a.y, b.x, b.y}; return __builtin_bit_cast(bf16x8, t); }
__device__ __forceinline__ bf16x8 as8(v4u t) { return __builtin_bit_cast(bf16x8, t); }
__device__ __forceinline__ f32x4 mfma16(bf16x8 a, bf16x8 b, f32x4 c) { return __builtin_amdgcn_mfma_f32_16x16x32_bf16(a, b, c, 0, 0, 0); }

__device__ __forceinline__ void layernorm_row(const u16* __restrict__ src, u16* __restrict__ dst, int lane) {
    v4u raw = *(const v4u*)(src + lane * 8);
    float x[8];
#pragma unroll
    for (int i = 0; i < 4; ++i) { x[2 * i] = __uint_as_float(raw[i] << 16); x[2 * i + 1] = __uint_as_float(raw[i] & 0xFFFF0000u); }
    float s = 0.f;
#pragma unroll
    for (int i = 0; i < 8; ++i) s += x[i];
#pragma unroll
    for (int o = 1; o < 64; o <<= 1) s += __shfl_xor(s, o);
    const float mean = s * (1.0f / DM);
    float q = 0.f;
#pragma unroll
    for (int i = 0; i < 8; ++i) { float d = x[i] - mean; q += d * d; }
#pragma unroll
    for (int o = 1; o < 64; o <<= 1) q += __shfl_xor(q, o);
    const float mb = bfr(mean);
    const float vb = bfr(q * (1.0f / DM));
    const float den = bfr(sqrtf(bfr(vb + 1e-5f)));
    v4u outv;
#pragma unroll
    for (int i = 0; i < 4; ++i) {
        float a = bfr(x[2 * i] - mb) / den, b = bfr(x[2 * i + 1] - mb) / den;
        outv[i] = pack2(a, b);
    }
    *(v4u*)(dst + lane * 8) = outv;
}

__global__ void __launch_bounds__(256) k_prep(const u16* __restrict__ x, u16* __restrict__ xn, const u16* __restrict__ mem0, u16* __restrict__ memx) {
    const int wave = threadIdx.x >> 6, lane = threadIdx.x & 63;
    const int row = blockIdx.x * 4 + wave;
    if (row < S_) layernorm_row(x + (size_t)row * DM, xn + (size_t)row * DM, lane);
    if (blockIdx.x == 0) {
        for (int i = threadIdx.x; i < MEMN * DM; i += 256) {
            int k = i >> 3, r = i & 7;
            u16 v = mem0[r * DM + k];
            if (v == 0xFFFFu) v = 0x7FFFu;
            memx[i] = v;
        }
    }
}

__global__ void __launch_bounds__(256) k_ln(const u16* __restrict__ x, u16* __restrict__ xn) {
    const int wave = threadIdx.x >> 6, lane = threadIdx.x & 63;
    const int row = blockIdx.x * 4 + wave;
    if (row < S_) layernorm_row(x + (size_t)row * DM, xn + (size_t)row * DM, lane);
}

struct GemmArgs {
    const u16* A; const u16* B0; const u16* B1; const u16* B2; const u16* res;
    u16* C0; u16* C1; u16* C2;
    int lda, ldb, K, pad;
};

__device__ __forceinline__ unsigned offB(unsigned row, unsigned ch) { return 256u * row + 16u * (ch ^ (((row & 3) << 2) | ((row >> 2) & 3))); }
__device__ __forceinline__ unsigned offA(unsigned row, unsigned ch) { return 128u * row + 16u * (ch ^ (row & 7)); }

template <int MODE, int BM>
__global__ void __launch_bounds__(256) k_gemm(GemmArgs g) {
    constexpr int BN = 128, BK = 64;
    constexpr int MT = BM / 32;
    constexpr int NB = (MODE == 2) ? 2 : 1;
    __shared__ __attribute__((aligned(16))) unsigned char sA[2][BM * BK * 2];
    __shared__ __attribute__((aligned(16))) unsigned char sB[2][NB][BK * BN * 2];
    const int tid = threadIdx.x, lane = tid & 63, wave = tid >> 6;
    const int wm = wave >> 1, wn = wave & 1;
    const int bm = blockIdx.y * BM;
    int bn = blockIdx.x * BN;
    const u16* Bp[NB];
    int sel = 0;
    if (MODE == 0) { sel = bn / HD; bn -= sel * HD; Bp[0] = sel == 0 ? g.B0 : (sel == 1 ? g.B1 : g.B2); }
    else if (MODE == 2) { Bp[0] = g.B0; Bp[NB - 1] = g.B1; }
    else Bp[0] = g.B0;

    f32x4 acc[NB][MT][4];
#pragma unroll
    for (int q = 0; q < NB; ++q)
#pragma unroll
        for (int i = 0; i < MT; ++i)
#pragma unroll
            for (int j = 0; j < 4; ++j) acc[q][i][j] = (f32x4){0.f, 0.f, 0.f, 0.f};

    constexpr int AL = BM * 8 / 256;
    v4u ra[AL], rb[NB][4];
    auto gload = [&](int k0) {
#pragma unroll
        for (int i = 0; i < AL; ++i) { int c = tid + 256 * i; int row = c >> 3, ch = c & 7; ra[i] = *(const v4u*)(g.A + (size_t)(bm + row) * g.lda + k0 + ch * 8); }
#pragma unroll
        for (int q = 0; q < NB; ++q)
#pragma unroll
            for (int i = 0; i < 4; ++i) { int c = tid + 256 * i; int row = c >> 4, ch = c & 15; rb[q][i] = *(const v4u*)(Bp[q] + (size_t)(k0 + row) * g.ldb + bn + ch * 8); }
    };
    auto swrite = [&](int buf) {
#pragma unroll
        for (int i = 0; i < AL; ++i) { int c = tid + 256 * i; int row = c >> 3, ch = c & 7; *(v4u*)(sA[buf] + offA(row, ch)) = ra[i]; }
#pragma unroll
        for (int q = 0; q < NB; ++q)
#pragma unroll
            for (int i = 0; i < 4; ++i) { int c = tid + 256 * i; int row = c >> 4, ch = c & 15; *(v4u*)(sB[buf][q] + offB(row, ch)) = rb[q][i]; }
    };
    const int nk = g.K / BK;
    gload(0); swrite(0);
    __syncthreads();
    const int G = lane >> 4, l15 = lane & 15, tq = l15 >> 2, tp = lane & 3;
    for (int kt = 0; kt < nk; ++kt) {
        const int buf = kt & 1;
        if (kt + 1 < nk) gload((kt + 1) * BK);
#pragma unroll
        for (int ks = 0; ks < 2; ++ks) {
            bf16x8 af[MT];
#pragma unroll
            for (int i = 0; i < MT; ++i) { int row = wm * (BM / 2) + i * 16 + l15; af[i] = as8(*(const v4u*)(sA[buf] + offA(row, ks * 4 + G))); }
#pragma unroll
            for (int q = 0; q < NB; ++q) {
#pragma unroll
                for (int j = 0; j < 4; ++j) {
                    const int c = wn * 4 + j;
                    v2u lo = tr_read(sB[buf][q] + offB(32 * ks + 8 * G + tq, 2 * c + (tp >> 1)) + 8 * (tp & 1));
                    v2u hi = tr_read(sB[buf][q] + offB(32 * ks + 8 * G + 4 + tq, 2 * c + (tp >> 1)) + 8 * (tp & 1));
                    bf16x8 bfr8 = mk8(lo, hi);
#pragma unroll
                    for (int i = 0; i < MT; ++i) acc[q][i][j] = mfma16(af[i], bfr8, acc[q][i][j]);
                }
            }
        }
        if (kt + 1 < nk) swrite(buf ^ 1);
        __syncthreads();
    }
#pragma unroll
    for (int i = 0; i < MT; ++i) {
#pragma unroll
        for (int j = 0; j < 4; ++j) {
            const int col = bn + (wn * 4 + j) * 16 + l15;
            const int row0 = bm + wm * (BM / 2) + i * 16 + 4 * G;
            if (MODE == 0) {
                if (sel < 2) {
                    u16* C = sel == 0 ? g.C0 : g.C1;
#pragma unroll
                    for (int r = 0; r < 4; ++r) C[(size_t)(row0 + r) * HD + col] = f2bf(acc[0][i][j][r]);
                } else {
                    const int seg = row0 >> 5, tok = row0 & 31;
                    v2u v = {pack2(acc[0][i][j][0], acc[0][i][j][1]), pack2(acc[0][i][j][2], acc[0][i][j][3])};
                    *(v2u*)(g.C2 + ((size_t)seg * HD + col) * 32 + tok) = v;
                }
            } else if (MODE == 1 || MODE == 3) {
#pragma unroll
                for (int r = 0; r < 4; ++r) {
                    size_t o = (size_t)(row0 + r) * DM + col;
                    g.C0[o] = f2bf(bf2f(g.res[o]) + bfr(acc[0][i][j][r]));
                }
            } else {
#pragma unroll
                for (int r = 0; r < 4; ++r) g.C0[(size_t)(row0 + r) * FF + col] = f2bf(bfr(acc[0][i][j][r]) * bfr(acc[NB - 1][i][j][r]));
            }
        }
    }
}

__global__ void __launch_bounds__(256) k_attn(const u16* __restrict__ Q, const u16* __restrict__ K, const u16* __restrict__ VT,
                                              const u16* __restrict__ KM, const u16* __restrict__ VMT, u16* __restrict__ O) {
    const int wave = threadIdx.x >> 6, lane = threadIdx.x & 63;
    const int id = blockIdx.x * 4 + wave;
    const int seg = id >> 5, h = id & 31;
    const int G = lane >> 4, l15 = lane & 15;
    const float scale = 0.08838834764831845f;
    f32x4 st[3][2];
#pragma unroll
    for (int a = 0; a < 3; ++a) { st[a][0] = (f32x4){0.f, 0.f, 0.f, 0.f}; st[a][1] = st[a][0]; }
#pragma unroll
    for (int ks = 0; ks < 4; ++ks) {
        const int eo = h * DH + ks * 32 + 8 * G;
        bf16x8 k0 = as8(*(const v4u*)(KM + ((size_t)seg * 8 + (l15 & 7)) * HD + eo));
        bf16x8 k1 = as8(*(const v4u*)(K + ((size_t)seg * 32 + l15) * HD + eo));
        bf16x8 k2 = as8(*(const v4u*)(K + ((size_t)seg * 32 + 16 + l15) * HD + eo));
        bf16x8 q0 = as8(*(const v4u*)(Q + ((size_t)seg * 32 + l15) * HD + eo));
        bf16x8 q1 = as8(*(const v4u*)(Q + ((size_t)seg * 32 + 16 + l15) * HD + eo));
        st[0][0] = mfma16(k0, q0, st[0][0]); st[0][1] = mfma16(k0, q1, st[0][1]);
        st[1][0] = mfma16(k1, q0, st[1][0]); st[1][1] = mfma16(k1, q1, st[1][1]);
        st[2][0] = mfma16(k2, q0, st[2][0]); st[2][1] = mfma16(k2, q1, st[2][1]);
    }
    bf16x8 pf[2][2];
#pragma unroll
    for (int qt = 0; qt < 2; ++qt) {
        const int t = qt * 16 + l15;
        float v[3][4];
        float m = -3.0e38f;
#pragma unroll
        for (int a = 0; a < 3; ++a)
#pragma unroll
            for (int r = 0; r < 4; ++r) {
                float s = bfr(bfr(st[a][qt][r]) * scale);
                bool ok;
                if (a == 0) ok = (G < 2); else ok = ((a - 1) * 16 + 4 * G + r) <= t;
                bool exist = (a != 0) || (G < 2);
                s = ok ? s : bfr(-1.0e9f);
                v[a][r] = exist ? s : -3.0e38f;
                m = fmaxf(m, v[a][r]);
            }
        m = fmaxf(m, __shfl_xor(m, 16)); m = fmaxf(m, __shfl_xor(m, 32));
        float sum = 0.f;
#pragma unroll
        for (int a = 0; a < 3; ++a)
#pragma unroll
            for (int r = 0; r < 4; ++r) { bool exist = (a != 0) || (G < 2); v[a][r] = exist ? __expf(v[a][r] - m) : 0.f; sum += v[a][r]; }
        sum += __shfl_xor(sum, 16); sum += __shfl_xor(sum, 32);
        const float inv = 1.0f / sum;
        v4u p0 = {pack2(v[0][0] * inv, v[0][1] * inv), pack2(v[0][2] * inv, v[0][3] * inv), pack2(v[1][0] * inv, v[1][1] * inv), pack2(v[1][2] * inv, v[1][3] * inv)};
        v4u p1 = {pack2(v[2][0] * inv, v[2][1] * inv), pack2(v[2][2] * inv, v[2][3] * inv), 0u, 0u};
        pf[qt][0] = as8(p0); pf[qt][1] = as8(p1);
    }
#pragma unroll
    for (int et = 0; et < 8; ++et) {
        const int e = et * 16 + l15;
        v2u vm = *(const v2u*)(VMT + (((size_t)seg * NH + h) * DH + e) * 8 + 4 * (G & 1));
        const u16* vx = VT + (((size_t)seg * NH + h) * DH + e) * 32;
        v2u v1 = *(const v2u*)(vx + 4 * G);
        v2u v2 = *(const v2u*)(vx + 16 + 4 * G);
        bf16x8 a0 = mk8(vm, v1);
        bf16x8 a1 = mk8(v2, (v2u){0u, 0u});
#pragma unroll
        for (int qt = 0; qt < 2; ++qt) {
            f32x4 o = (f32x4){0.f, 0.f, 0.f, 0.f};
            o = mfma16(a0, pf[qt][0], o);
            o = mfma16(a1, pf[qt][1], o);
            v2u ov = {pack2(o[0], o[1]), pack2(o[2], o[3])};
            *(v2u*)(O + ((size_t)seg * 32 + qt * 16 + l15) * HD + h * DH + et * 16 + 4 * G) = ov;
        }
    }
}


__global__ void __launch_bounds__(256) k_pack(const u16* __restrict__ Wq, const u16* __restrict__ Wk, const u16* __restrict__ Wv,
                                              const u16* __restrict__ Wo, u16* __restrict__ WQKF, u16* __restrict__ WVF, u16* __restrict__ WOF) {
    const int gid = blockIdx.x * 256 + threadIdx.x;
    constexpr int NQK = 32 * 8 * 16 * 2 * 64, NV = 64 * 4 * 16 * 64, NO = 64 * 32 * 2 * 64;
    u16 v[8];
    u16* dst;
    if (gid < NQK) {
        const int ln = gid & 63, half = (gid >> 6) & 1, t = (gid >> 7) & 15, w = (gid >> 11) & 7, h = gid >> 14;
        const int gb = ln >> 4, e = h * DH + w * 16 + (ln & 15);
#pragma unroll
        for (int j = 0; j < 8; ++j) {
            const int jb = half * 8 + j;
            const int kr = 16 * (jb >> 3) + 4 * gb + 2 * ((jb >> 2) & 1) + (jb & 1);
            const u16* W = ((jb >> 1) & 1) ? Wk : Wq;
            v[j] = W[(size_t)(32 * t + kr) * HD + e];
        }
        dst = WQKF + (size_t)gid * 8;
    } else if (gid < NQK + NV) {
        const int i = gid - NQK;
        const int ln = i & 63, t = (i >> 6) & 15, vt = (i >> 10) & 3, b = i >> 12;
        const int e = (b >> 1) * DH + 64 * (b & 1) + vt * 16 + (ln & 15);
#pragma unroll
        for (int j = 0; j < 8; ++j) v[j] = Wv[(size_t)(32 * t + 8 * (ln >> 4) + j) * HD + e];
        dst = WVF + (size_t)i * 8;
    } else if (gid < NQK + NV + NO) {
        const int i = gid - NQK - NV;
        const int ln = i & 63, half = (i >> 6) & 1, f = (i >> 7) & 31, b = i >> 12;
        const int dp = f >> 1, kk = f & 1, gb = ln >> 4, c15 = ln & 15;
#pragma unroll
        for (int j = 0; j < 8; ++j) {
            const int jb = half * 8 + j;
            const int kr = 16 * (jb >> 3) + 4 * gb + 2 * ((jb >> 2) & 1) + (jb & 1);
            const int el = 32 * kk + 16 * ((kr & 7) >> 2) + 4 * (kr >> 3) + (kr & 3);
            const int d = 16 * (2 * dp + ((jb >> 1) & 1)) + c15;
            v[j] = Wo[(size_t)((b >> 1) * DH + 64 * (b & 1) + el) * DM + d];
        }
        dst = WOF + (size_t)i * 8;
    } else return;
    v4u o = {(unsigned)v[0] | ((unsigned)v[1] << 16), (unsigned)v[2] | ((unsigned)v[3] << 16), (unsigned)v[4] | ((unsigned)v[5] << 16), (unsigned)v[6] | ((unsigned)v[7] << 16)};
    *(v4u*)dst = o;
}

constexpr int NX = 64;
constexpr int MEMX_STEP = 8192;
constexpr int PART_BUF = 64 * 64 * 128;
constexpr unsigned SPIN_CAP = 1u << 22;

struct RecArgs {
    const u16* WQKF; const u16* WVF; const u16* WOF;
    const u16* K; const u16* VT;
    u16* KM; u16* VMT;
    unsigned char* memx; unsigned char* part;
};

constexpr int L_WO = 0;
constexpr int L_MEM = 65536;
constexpr int L_T = L_MEM + 8192;
constexpr int L_VM = L_T + 4096;
constexpr int L_TOTAL = 96 * 1024;

__device__ __forceinline__ bool valid16(v4u v) { return (v.x != 0xFFFFFFFFu) & (v.y != 0xFFFFFFFFu) & (v.z != 0xFFFFFFFFu) & (v.w != 0xFFFFFFFFu); }

__global__ void __launch_bounds__(512, 2) k_rec(RecArgs a) {
    extern __shared__ __attribute__((aligned(16))) unsigned char lds[];
    const int tid = threadIdx.x, lane = tid & 63;
    const int wave = __builtin_amdgcn_readfirstlane(tid >> 6);
    const int G = lane >> 4, l15 = lane & 15;
    const __amdgpu_buffer_rsrc_t rmem = __builtin_amdgcn_make_buffer_rsrc(a.memx, 0, (NSEG + 1) * MEMX_STEP, 0x00020000);
    const __amdgpu_buffer_rsrc_t rpart = __builtin_amdgcn_make_buffer_rsrc(a.part, 0, 2 * PART_BUF, 0x00020000);

    if (blockIdx.x >= NX) {
        if (wave != 0) return;
        const int y = blockIdx.x - NX;
        for (int s = 0; s < NSEG; ++s) {
            const unsigned base = (s & 1) * PART_BUF + y * 8192 + lane * 16;
            v4u m[8];
            unsigned spins = 0;
            for (;;) {
                bool ok = true;
#pragma unroll
                for (int t = 0; t < 8; ++t) m[t] = __builtin_amdgcn_raw_buffer_load_b128(rpart, base + t * 1024, 0, 16);
#pragma unroll
                for (int t = 0; t < 8; ++t) ok &= valid16(m[t]);
                asm volatile("" ::: "memory");
                if (__all(ok)) break;
                if (++spins > SPIN_CAP) break;
            }
            float sum[8];
#pragma unroll
            for (int r = 0; r < 8; ++r) sum[r] = 0.f;
#pragma unroll
            for (int t = 0; t < 8; ++t)
#pragma unroll
                for (int i = 0; i < 4; ++i) { sum[2 * i] += __uint_as_float(m[t][i] << 16); sum[2 * i + 1] += __uint_as_float(m[t][i] & 0xFFFF0000u); }
#pragma unroll
            for (int r = 0; r < 8; ++r) { sum[r] += __shfl_xor(sum[r], 8); sum[r] += __shfl_xor(sum[r], 16); sum[r] += __shfl_xor(sum[r], 32); }
            if (lane < 8) {
                v4u v = {nosent(pack2(sum[0], sum[1])), nosent(pack2(sum[2], sum[3])), nosent(pack2(sum[4], sum[5])), nosent(pack2(sum[6], sum[7]))};
                __builtin_amdgcn_raw_buffer_store_b128(v, rmem, (s + 1) * MEMX_STEP + y * 128 + lane * 16, 0, 16);
            }
            const v4u sent = {0xFFFFFFFFu, 0xFFFFFFFFu, 0xFFFFFFFFu, 0xFFFFFFFFu};
#pragma unroll
            for (int t = 0; t < 8; ++t) __builtin_amdgcn_raw_buffer_store_b128(sent, rpart, base + t * 1024, 0, 16);
            asm volatile("s_waitcnt vmcnt(0)" ::: "memory");
        }
        return;
    }

    const int b = blockIdx.x, h = b >> 1, g = b & 1;
    bf16x16 wqk[16];
    {
        const u16* src = a.WQKF + ((size_t)(h * 8 + wave) * 16 * 2 * 64 + lane) * 8;
#pragma unroll
        for (int t = 0; t < 16; ++t) {
            bf16x8 lo = as8(*(const v4u*)(src + (size_t)(t * 2) * 512));
            bf16x8 hi = as8(*(const v4u*)(src + (size_t)(t * 2 + 1) * 512));
#pragma unroll
            for (int j = 0; j < 8; ++j) { wqk[t][j] = lo[j]; wqk[t][8 + j] = hi[j]; }
        }
    }
    for (int idx = tid; idx < 4096; idx += 512) *(v4u*)(lds + L_WO + idx * 16) = *(const v4u*)(a.WOF + ((size_t)b * 4096 + idx) * 8);
    const int sidx = (lane & 8) ? 0xEEEE : 0x4444;
    const int tq = l15 >> 2, tp = lane & 3;
    const unsigned char* memrd = lds + L_MEM + (8 * G + tq) * 16 + (tp & 1) * 8;

    auto poll_mem = [&](int s) {
        v4u m;
        unsigned spins = 0;
        for (;;) {
            m = __builtin_amdgcn_raw_buffer_load_b128(rmem, s * MEMX_STEP + tid * 16, 0, 16);
            asm volatile("" ::: "memory");
            if (__all(valid16(m))) break;
            if (++spins > SPIN_CAP) break;
        }
        *(v4u*)(lds + L_MEM + tid * 16) = m;
    };
    auto qk_gemm = [&]() {
        f32x4 acc = (f32x4){0.f, 0.f, 0.f, 0.f};
#pragma unroll
        for (int t = 0; t < 16; ++t) {
            v2u lo = tr_read(memrd + t * 512);
            v2u hi = tr_read(memrd + t * 512 + 64);
            acc = __builtin_amdgcn_smfmac_f32_16x16x64_bf16(mk8(lo, hi), wqk[t], acc, sidx, 0, 0);
        }
        v2u v = {pack2(acc[0], acc[1]), pack2(acc[2], acc[3])};
        *(v2u*)(lds + L_T + (wave * 16 + l15) * 32 + G * 8) = v;
    };

    if (wave < 4) {
        const float scale = 0.08838834764831845f;
        for (int s = 0; s < NSEG; ++s) {
            v4u kx[2][4]; v2u vx[4][2];
#pragma unroll
            for (int kt = 0; kt < 2; ++kt)
#pragma unroll
                for (int ks = 0; ks < 4; ++ks) kx[kt][ks] = *(const v4u*)(a.K + ((size_t)s * 32 + kt * 16 + l15) * HD + h * DH + ks * 32 + 8 * G);
#pragma unroll
            for (int et = 0; et < 4; ++et) {
                const u16* p = a.VT + (((size_t)s * NH + h) * DH + 64 * g + et * 16 + l15) * 32;
                vx[et][0] = *(const v2u*)(p + 4 * G); vx[et][1] = *(const v2u*)(p + 16 + 4 * G);
            }
            poll_mem(s);
            __syncthreads();
            qk_gemm();
            __syncthreads();
            f32x4 st[3];
#pragma unroll
            for (int i = 0; i < 3; ++i) st[i] = (f32x4){0.f, 0.f, 0.f, 0.f};
#pragma unroll
            for (int ks = 0; ks < 4; ++ks) {
                const unsigned char* tb = lds + L_T + (ks * 32 + 8 * G + tq) * 32 + (tp & 1) * 8;
                bf16x8 qf = mk8(tr_read(tb), tr_read(tb + 128));
                v2u k_lo = tr_read(tb + 16), k_hi = tr_read(tb + 128 + 16);
                bf16x8 kf = mk8(k_lo, k_hi);
                st[0] = mfma16(kf, qf, st[0]);
                st[1] = mfma16(as8(kx[0][ks]), qf, st[1]);
                st[2] = mfma16(as8(kx[1][ks]), qf, st[2]);
                if (wave == 0 && g == 0 && l15 < 8) {
                    v4u kv = {k_lo.x, k_lo.y, k_hi.x, k_hi.y};
                    *(v4u*)(a.KM + ((size_t)s * 8 + l15) * HD + h * DH + ks * 32 + 8 * G) = kv;
                }
            }
            float v[3][4];
            float m = -3.0e38f;
#pragma unroll
            for (int i = 0; i < 3; ++i)
#pragma unroll
                for (int r = 0; r < 4; ++r) {
                    float sc = bfr(bfr(st[i][r]) * scale);
                    bool exist = (i != 0) || (G < 2);
                    v[i][r] = exist ? sc : -3.0e38f;
                    m = fmaxf(m, v[i][r]);
                }
            m = fmaxf(m, __shfl_xor(m, 16)); m = fmaxf(m, __shfl_xor(m, 32));
            float sum = 0.f;
#pragma unroll
            for (int i = 0; i < 3; ++i)
#pragma unroll
                for (int r = 0; r < 4; ++r) { bool exist = (i != 0) || (G < 2); v[i][r] = exist ? __expf(v[i][r] - m) : 0.f; sum += v[i][r]; }
            sum += __shfl_xor(sum, 16); sum += __shfl_xor(sum, 32);
            const float inv = 1.0f / sum;
            v4u p0 = {pack2(v[0][0] * inv, v[0][1] * inv), pack2(v[0][2] * inv, v[0][3] * inv), pack2(v[1][0] * inv, v[1][1] * inv), pack2(v[1][2] * inv, v[1][3] * inv)};
            v4u p1 = {pack2(v[2][0] * inv, v[2][1] * inv), pack2(v[2][2] * inv, v[2][3] * inv), 0u, 0u};
            __syncthreads();
            v2u ob[4];
#pragma unroll
            for (int et = 0; et < 4; ++et) {
                v2u vm = *(const v2u*)(lds + L_VM + et * 512 + lane * 8);
                f32x4 o = (f32x4){0.f, 0.f, 0.f, 0.f};
                o = mfma16(mk8(vm, vx[et][0]), as8(p0), o);
                o = mfma16(mk8(vx[et][1], (v2u){0u, 0u}), as8(p1), o);
                ob[et] = (v2u){pack2(o[0], o[1]), pack2(o[2], o[3])};
            }
            const bf16x8 oa0 = mk8(ob[0], ob[1]), oa1 = mk8(ob[2], ob[3]);
#pragma unroll
            for (int i = 0; i < 4; ++i) {
                const int dp = wave * 4 + i;
                f32x4 d = (f32x4){0.f, 0.f, 0.f, 0.f};
#pragma unroll
                for (int kk = 0; kk < 2; ++kk) {
                    const unsigned char* wp = lds + L_WO + ((dp * 2 + kk) * 2) * 1024 + lane * 16;
                    v4u w0 = *(const v4u*)wp, w1 = *(const v4u*)(wp + 1024);
                    bf16x16 wf;
                    bf16x8 w0b = as8(w0), w1b = as8(w1);
#pragma unroll
                    for (int j = 0; j < 8; ++j) { wf[j] = w0b[j]; wf[8 + j] = w1b[j]; }
                    d = __builtin_amdgcn_smfmac_f32_16x16x64_bf16(kk == 0 ? oa0 : oa1, wf, d, sidx, 0, 0);
                }
                const int dcol = 32 * dp + 16 * (G >> 1) + l15;
                v2u pv = {nosent(pack2(d[0], d[1])), nosent(pack2(d[2], d[3]))};
                __builtin_amdgcn_raw_buffer_store_b64(pv, rpart, (s & 1) * PART_BUF + (dcol >> 3) * 8192 + b * 128 + (dcol & 7) * 16 + (G & 1) * 8, 0, 16);
            }
        }
    } else {
        const int vt = wave - 4;
        bf16x8 wv[16];
        {
            const u16* src = a.WVF + ((size_t)(b * 4 + vt) * 16 * 64 + lane) * 8;
#pragma unroll
            for (int t = 0; t < 16; ++t) wv[t] = as8(*(const v4u*)(src + (size_t)t * 512));
        }
        for (int s = 0; s < NSEG; ++s) {
            poll_mem(s);
            __syncthreads();
            qk_gemm();
            __syncthreads();
            f32x4 acc = (f32x4){0.f, 0.f, 0.f, 0.f};
#pragma unroll
            for (int t = 0; t < 16; ++t) {
                v2u lo = tr_read(memrd + t * 512);
                v2u hi = tr_read(memrd + t * 512 + 64);
                acc = mfma16(mk8(lo, hi), wv[t], acc);
            }
            v2u v = {pack2(acc[0], acc[1]), pack2(acc[2], acc[3])};
            *(v2u*)(lds + L_VM + vt * 512 + lane * 8) = v;
            if (G < 2) *(v2u*)(a.VMT + (((size_t)s * NH + h) * DH + 64 * g + vt * 16 + l15) * 8 + 4 * G) = v;
            __syncthreads();
        }
    }
}

extern "C" void kernel_launch(void* const* d_in, const int* in_sizes, int n_in, void* d_out, int out_size, void* d_ws, size_t ws_size,
                              hipStream_t stream) {
    const u16* x = (const u16*)d_in[0];
    const u16* mem0 = (const u16*)d_in[1];
    const u16* Wq = (const u16*)d_in[2];
    const u16* Wk = (const u16*)d_in[3];
    const u16* Wv = (const u16*)d_in[4];
    const u16* Wo = (const u16*)d_in[5];
    const u16* Wg = (const u16*)d_in[8];
    const u16* Wu = (const u16*)d_in[9];
    const u16* Wd = (const u16*)d_in[10];
    u16* out = (u16*)d_out;

    unsigned char* ws = (unsigned char*)d_ws;
    size_t off = 0;
    auto alloc = [&](size_t bytes) { unsigned char* p = ws + off; off += (bytes + 255) & ~(size_t)255; return p; };
    unsigned char* xchg = alloc((size_t)2 * PART_BUF + (size_t)(NSEG + 1) * MEMX_STEP);
    unsigned char* part = xchg;
    unsigned char* memx = xchg + 2 * PART_BUF;
    u16* xn = (u16*)alloc((size_t)S_ * DM * 2);
    u16* Q = (u16*)alloc((size_t)S_ * HD * 2);
    u16* K = (u16*)alloc((size_t)S_ * HD * 2);
    u16* VT = (u16*)alloc((size_t)S_ * HD * 2);
    u16* KM = (u16*)alloc((size_t)NSEG * 8 * HD * 2);
    u16* VMT = (u16*)alloc((size_t)NSEG * 8 * HD * 2);
    u16* O = (u16*)alloc((size_t)S_ * HD * 2);
    u16* hb = (u16*)alloc((size_t)S_ * DM * 2);
    u16* hn = (u16*)alloc((size_t)S_ * DM * 2);
    u16* act = (u16*)alloc((size_t)S_ * FF * 2);
    u16* WQKF = (u16*)alloc((size_t)2 * DM * HD * 2);
    u16* WVF = (u16*)alloc((size_t)DM * HD * 2);
    u16* WOF = (u16*)alloc((size_t)DM * HD * 2);

    (void)hipMemsetAsync(xchg, 0xFF, (size_t)2 * PART_BUF + (size_t)(NSEG + 1) * MEMX_STEP, stream);
    k_prep<<<S_ / 4, 256, 0, stream>>>(x, xn, mem0, (u16*)memx);
    k_pack<<<4 * DM * HD / 8 / 256, 256, 0, stream>>>(Wq, Wk, Wv, Wo, WQKF, WVF, WOF);
    {
        GemmArgs g{xn, Wq, Wk, Wv, nullptr, Q, K, VT, DM, HD, DM, 0};
        k_gemm<0, 128><<<dim3(3 * HD / 128, S_ / 128), 256, 0, stream>>>(g);
    }
    {
        static bool attr_set = false;
        if (!attr_set) { (void)hipFuncSetAttribute((const void*)k_rec, hipFuncAttributeMaxDynamicSharedMemorySize, L_TOTAL); attr_set = true; }
        RecArgs r{WQKF, WVF, WOF, K, VT, KM, VMT, memx, part};
        k_rec<<<2 * NX, 512, L_TOTAL, stream>>>(r);
    }
    k_attn<<<NSEG * NH / 4, 256, 0, stream>>>(Q, K, VT, KM, VMT, O);
    {
        GemmArgs g{O, Wo, nullptr, nullptr, x, hb, nullptr, nullptr, HD, DM, HD, 0};
        k_gemm<1, 64><<<dim3(DM / 128, S_ / 64), 256, 0, stream>>>(g);
    }
    k_ln<<<S_ / 4, 256, 0, stream>>>(hb, hn);
    {
        GemmArgs g{hn, Wg, Wu, nullptr, nullptr, act, nullptr, nullptr, DM, FF, DM, 0};
        k_gemm<2, 64><<<dim3(FF / 128, S_ / 64), 256, 0, stream>>>(g);
    }
    {
        GemmArgs g{act, Wd, nullptr, nullptr, hb, out, nullptr, nullptr, FF, DM, FF, 0};
        k_gemm<3, 64><<<dim3(DM / 128, S_ / 64), 256, 0, stream>>>(g);
    }
}
```

```cpp
#include <hip/hip_runtime.h>
#include <hip/hip_bf16.h>

typedef unsigned short u16;
typedef __bf16 bf16x8 __attribute__((ext_vector_type(8)));
typedef __bf16 bf16x16 __attribute__((ext_vector_type(16)));
typedef float f32x4 __attribute__((ext_vector_type(4)));
typedef unsigned v4u __attribute__((ext_vector_type(4)));
typedef unsigned v2u __attribute__((ext_vector_type(2)));
typedef short v4s __attribute__((ext_vector_type(4)));
#define LAS __attribute__((address_space(3)))

constexpr int S_ = 2048, DM = 512, NH = 32, DH = 128, HD = NH * DH, MEMN = 8, SEG = 32, NSEG = 64, FF = 1792;

__device__ __forceinline__ float bf2f(u16 v) { return __uint_as_float(((unsigned)v) << 16); }
__device__ __forceinline__ u16 f2bf(float f) { return __builtin_bit_cast(u16, (__bf16)f); }
__device__ __forceinline__ float bfr(float f) { return bf2f(f2bf(f)); }
typedef float f32x2 __attribute__((ext_vector_type(2)));
typedef __bf16 bf16x2 __attribute__((ext_vector_type(2)));
__device__ __forceinline__ unsigned pack2(float a, float b) { f32x2 t = {a, b}; return __builtin_bit_cast(unsigned, __builtin_convertvector(t, bf16x2)); }
__device__ __forceinline__ float lo16f(unsigned w) { return __uint_as_float(w << 16); }
__device__ __forceinline__ float hi16f(unsigned w) { return __uint_as_float(w & 0xFFFF0000u); }
template <int CTRL> __device__ __forceinline__ float dppf(float x) {
    return __builtin_bit_cast(float, __builtin_amdgcn_mov_dpp(__builtin_bit_cast(int, x), CTRL, 0xf, 0xf, true));
}
__device__ __forceinline__ float xrow16_max(float x) {
    auto s = __builtin_amdgcn_permlane16_swap(__float_as_uint(x), __float_as_uint(x), false, false);
    x = fmaxf(__uint_as_float(s[0]), __uint_as_float(s[1]));
    auto t = __builtin_amdgcn_permlane32_swap(__float_as_uint(x), __float_as_uint(x), false, false);
    return fmaxf(__uint_as_float(t[0]), __uint_as_float(t[1]));
}
__device__ __forceinline__ float xrow16_sum(float x) {
    auto s = __builtin_amdgcn_permlane16_swap(__float_as_uint(x), __float_as_uint(x), false, false);
    x = __uint_as_float(s[0]) + __uint_as_float(s[1]);
    auto t = __builtin_amdgcn_permlane32_swap(__float_as_uint(x), __float_as_uint(x), false, false);
    return __uint_as_float(t[0]) + __uint_as_float(t[1]);
}
__device__ __forceinline__ unsigned umax3(unsigned a, unsigned b, unsigned c) { return max(max(a, b), c); }
__device__ __forceinline__ unsigned umax4(v4u v) { return max(umax3(v.x, v.y, v.z), v.w); }
__device__ __forceinline__ bool valid16(v4u v) { return umax4(v) != 0xFFFFFFFFu; }
#define SB0() __builtin_amdgcn_sched_barrier(0)
__device__ __forceinline__ void glds16(const void* gsrc, unsigned lds_dst) {
    unsigned keep;
    asm volatile("s_mov_b32 %0, m0\n\ts_mov_b32 m0, %2\n\ts_nop 0\n\tglobal_load_lds_dwordx4 %1, off\n\ts_mov_b32 m0, %0" : "=&s"(keep) : "v"(gsrc), "s"(lds_dst) : "memory");
}
__device__ __forceinline__ void glds16_sc1(const void* gsrc, unsigned lds_dst) {
    unsigned keep;
    asm volatile("s_mov_b32 %0, m0\n\ts_mov_b32 m0, %2\n\ts_nop 0\n\tglobal_load_lds_dwordx4 %1, off sc1\n\ts_mov_b32 m0, %0" : "=&s"(keep) : "v"(gsrc), "s"(lds_dst) : "memory");
}
__device__ __forceinline__ unsigned lds_addr(const void* p) { return (unsigned)(size_t)(LAS const unsigned char*)p; }
__device__ __forceinline__ unsigned nosent(unsigned w) { return w == 0xFFFFFFFFu ? 0x7FFF7FFFu : w; }
__device__ __forceinline__ v2u tr_read(const void* p) {
    return __builtin_bit_cast(v2u, __builtin_amdgcn_ds_read_tr16_b64_v4i16((LAS v4s*)p));
}
__device__ __forceinline__ bf16x8 mk8(v2u a, v2u b) { v4u t = {a.x, a.y, b.x, b.y}; return __builtin_bit_cast(bf16x8, t); }
__device__ __forceinline__ bf16x8 as8(v4u t) { return __builtin_bit_cast(bf16x8, t); }
__device__ __forceinline__ f32x4 mfma16(bf16x8 a, bf16x8 b, f32x4 c) { return __builtin_amdgcn_mfma_f32_16x16x32_bf16(a, b, c, 0, 0, 0); }

__device__ __forceinline__ float wave_sum(float s) {
    s += dppf<0xB1>(s); s += dppf<0x4E>(s); s += dppf<0x141>(s); s += dppf<0x128>(s);
    return xrow16_sum(s);
}
__device__ __forceinline__ v4u layernorm_vals(v4u raw) {
    float x[8];
#pragma unroll
    for (int i = 0; i < 4; ++i) { x[2 * i] = lo16f(raw[i]); x[2 * i + 1] = hi16f(raw[i]); }
    float s = ((x[0] + x[1]) + (x[2] + x[3])) + ((x[4] + x[5]) + (x[6] + x[7]));
    const float mean = wave_sum(s) * (1.0f / DM);
    float q = 0.f;
#pragma unroll
    for (int i = 0; i < 8; ++i) { float d = x[i] - mean; q += d * d; }
    q = wave_sum(q);
    const float mb = bfr(mean);
    const float vb = bfr(q * (1.0f / DM));
    const float den = bfr(sqrtf(bfr(vb + 1e-5f)));
    v4u outv;
#pragma unroll
    for (int i = 0; i < 4; ++i) {
        float a = bfr(x[2 * i] - mb) / den, b = bfr(x[2 * i + 1] - mb) / den;
        outv[i] = pack2(a, b);
    }
    return outv;
}
__device__ __forceinline__ void layernorm_row(const u16* __restrict__ src, u16* __restrict__ dst, int lane) {
    *(v4u*)(dst + lane * 8) = layernorm_vals(*(const v4u*)(src + lane * 8));
}

__global__ void __launch_bounds__(256) k_prep(const u16* __restrict__ x, u16* __restrict__ xn, const u16* __restrict__ mem0, u16* __restrict__ memx,
                                              v4u* __restrict__ sent, int n_sent16, int memx_off16, v4u* __restrict__ flags, int n_flag16) {
    const int wave = threadIdx.x >> 6, lane = threadIdx.x & 63;
    const int row = blockIdx.x * 4 + wave;
    if (row < S_) layernorm_row(x + (size_t)row * DM, xn + (size_t)row * DM, lane);
    const v4u ones = {0xFFFFFFFFu, 0xFFFFFFFFu, 0xFFFFFFFFu, 0xFFFFFFFFu};
    const int gt = blockIdx.x * 256 + threadIdx.x, nt = gridDim.x * 256;
    for (int i = gt; i < n_sent16; i += nt)
        if (i < memx_off16 || i >= memx_off16 + 512) sent[i] = ones;
    for (int i = gt; i < n_flag16; i += nt) flags[i] = (v4u){0u, 0u, 0u, 0u};
    if (blockIdx.x == 0) {
        for (int i = threadIdx.x; i < MEMN * DM; i += 256) {
            int k = i >> 3, r = i & 7;
            u16 v = mem0[r * DM + k];
            if (v == 0xFFFFu) v = 0x7FFFu;
            memx[i] = v;
        }
    }
}

struct GemmArgs {
    const u16* A; const u16* B0; const u16* B1; const u16* B2; const u16* res;
    u16* C0; u16* C1; u16* C2;
    int lda, ldb, K, pad;
};

__device__ __forceinline__ unsigned offB(unsigned row, unsigned ch) { return 256u * row + 16u * (ch ^ (((row & 3) << 2) | ((row >> 2) & 3))); }
__device__ __forceinline__ unsigned offA(unsigned row, unsigned ch) { return 128u * row + 16u * (ch ^ (row & 7)); }

__device__ __forceinline__ unsigned fB(unsigned row) { return (((row >> 1) & 1) | (((row >> 3) & 1) << 1)) << 1; }
template <int MODE, int BM, int NB, int NST>
__global__ void __launch_bounds__(256) k_gemm2(GemmArgs g) {
    extern __shared__ __attribute__((aligned(16))) unsigned char sm[];
    constexpr int MT = BM / 32;
    constexpr int ABYTES = BM * 128, STAGE = ABYTES + NB * 8192;
    constexpr int LA = BM / 32, LPS = LA + 2 * NB;
    const int tid = threadIdx.x, lane = tid & 63;
    const int wave = __builtin_amdgcn_readfirstlane(tid >> 6);
    const int wm = wave >> 1, wn = wave & 1;
    const int ncol = g.pad & 0xFFFF, nz = g.pad >> 16;
    const int per_xcd = gridDim.x >> 3;
    const int lin = (blockIdx.x & 7) * per_xcd + (blockIdx.x >> 3);
    const int panel = lin / ncol, bz = panel % nz;
    const int bm = (panel / nz) * BM, bn = (lin % ncol) * 64;
    const int kbase = bz * g.K;
    const u16* Asrc[LA];
    const u16* Bsrc[NB][2];
#pragma unroll
    for (int i = 0; i < LA; ++i) { int q = tid + 256 * i; int row = q >> 3, ch = (q & 7) ^ (row & 7); Asrc[i] = g.A + (size_t)(bm + row) * g.lda + kbase + ch * 8; }
#pragma unroll
    for (int n = 0; n < NB; ++n)
#pragma unroll
        for (int i = 0; i < 2; ++i) { int q = tid + 256 * i; int row = q >> 3, ch = (q & 7) ^ fB(row); Bsrc[n][i] = (n == 0 ? g.B0 : g.B1) + (size_t)(kbase + row) * g.ldb + bn + ch * 8; }
    const unsigned sm_base = __builtin_amdgcn_readfirstlane(lds_addr(sm)) + wave * 1024;
    auto stage = [&](int kt) {
        const unsigned base = sm_base + (kt % NST) * STAGE;
#pragma unroll
        for (int i = 0; i < LA; ++i) glds16(Asrc[i] + kt * 64, base + i * 4096);
#pragma unroll
        for (int n = 0; n < NB; ++n)
#pragma unroll
            for (int i = 0; i < 2; ++i) glds16(Bsrc[n][i] + (size_t)kt * 64 * g.ldb, base + ABYTES + n * 8192 + i * 4096);
    };
    f32x4 acc[NB][MT][2];
#pragma unroll
    for (int n = 0; n < NB; ++n)
#pragma unroll
        for (int i = 0; i < MT; ++i) { acc[n][i][0] = (f32x4){0.f, 0.f, 0.f, 0.f}; acc[n][i][1] = acc[n][i][0]; }
    const int nk = g.K / 64;
#pragma unroll
    for (int t = 0; t < NST - 1; ++t) stage(t);
    const int G = lane >> 4, l15 = lane & 15, tq = l15 >> 2, tp = lane & 3;
    for (int kt = 0; kt < nk; ++kt) {
        if (kt + NST - 1 <= nk) asm volatile("s_waitcnt vmcnt(%0)" :: "i"((NST - 2) * LPS) : "memory");
        else asm volatile("s_waitcnt vmcnt(0)" ::: "memory");
        asm volatile("s_waitcnt lgkmcnt(0)" ::: "memory");
        __builtin_amdgcn_s_barrier();
        if (kt + NST - 1 < nk) stage(kt + NST - 1);
        const unsigned char* sA = sm + (kt % NST) * STAGE;
        const unsigned char* sB = sA + ABYTES;
#pragma unroll
        for (int ks = 0; ks < 2; ++ks) {
            bf16x8 af[MT], bq[NB][2];
#pragma unroll
            for (int i = 0; i < MT; ++i) af[i] = as8(*(const v4u*)(sA + offA(wm * (BM / 2) + i * 16 + l15, ks * 4 + G)));
#pragma unroll
            for (int n = 0; n < NB; ++n)
#pragma unroll
                for (int j = 0; j < 2; ++j) {
                    const unsigned r0 = 32 * ks + 8 * G + tq, r1 = r0 + 4, ch = (wn * 2 + j) * 2 + (tp >> 1);
                    v2u lo = tr_read(sB + n * 8192 + r0 * 128 + ((ch ^ fB(r0)) * 16) + 8 * (tp & 1));
                    v2u hi = tr_read(sB + n * 8192 + r1 * 128 + ((ch ^ fB(r1)) * 16) + 8 * (tp & 1));
                    bq[n][j] = mk8(lo, hi);
                }
#pragma unroll
            for (int n = 0; n < NB; ++n)
#pragma unroll
                for (int i = 0; i < MT; ++i)
#pragma unroll
                    for (int j = 0; j < 2; ++j) acc[n][i][j] = mfma16(bq[n][j], af[i], acc[n][i][j]);
        }
    }
#pragma unroll
    for (int i = 0; i < MT; ++i)
#pragma unroll
        for (int j = 0; j < 2; ++j) {
            const int row = bm + wm * (BM / 2) + i * 16 + l15;
            const int col = bn + (wn * 2 + j) * 16 + 4 * G;
            if (MODE == 1) {
                float* P = (float*)g.C0 + ((size_t)bz * S_ + row) * DM + col;
                *(f32x4*)P = acc[0][i][j];
            } else if (MODE == 2) {
                unsigned g0 = pack2(acc[0][i][j][0], acc[0][i][j][1]), g1 = pack2(acc[0][i][j][2], acc[0][i][j][3]);
                unsigned u0 = pack2(acc[NB - 1][i][j][0], acc[NB - 1][i][j][1]), u1 = pack2(acc[NB - 1][i][j][2], acc[NB - 1][i][j][3]);
                v2u v = {pack2(lo16f(g0) * lo16f(u0), hi16f(g0) * hi16f(u0)), pack2(lo16f(g1) * lo16f(u1), hi16f(g1) * hi16f(u1))};
                *(v2u*)(g.C0 + (size_t)row * FF + col) = v;
            } else {
                v2u r = *(const v2u*)(g.res + (size_t)row * DM + col);
                unsigned a0 = pack2(acc[0][i][j][0], acc[0][i][j][1]), a1 = pack2(acc[0][i][j][2], acc[0][i][j][3]);
                v2u v = {pack2(lo16f(r.x) + lo16f(a0), hi16f(r.x) + hi16f(a0)), pack2(lo16f(r.y) + lo16f(a1), hi16f(r.y) + hi16f(a1))};
                *(v2u*)(g.C0 + (size_t)row * DM + col) = v;
            }
        }
}

template <int NZ>
__global__ void __launch_bounds__(256) k_res_ln(const float* __restrict__ P, const u16* __restrict__ x, u16* __restrict__ hb, u16* __restrict__ hn) {
    const int wave = threadIdx.x >> 6, lane = threadIdx.x & 63;
    const int row = blockIdx.x * 4 + wave;
    f32x4 s0 = *(const f32x4*)(P + (size_t)row * DM + lane * 8), s1 = *(const f32x4*)(P + (size_t)row * DM + lane * 8 + 4);
#pragma unroll
    for (int z = 1; z < NZ; ++z) { s0 += *(const f32x4*)(P + ((size_t)z * S_ + row) * DM + lane * 8); s1 += *(const f32x4*)(P + ((size_t)z * S_ + row) * DM + lane * 8 + 4); }
    v4u raw = *(const v4u*)(x + (size_t)row * DM + lane * 8);
    unsigned a0 = pack2(s0[0], s0[1]), a1 = pack2(s0[2], s0[3]), a2 = pack2(s1[0], s1[1]), a3 = pack2(s1[2], s1[3]);
    v4u hv = {pack2(lo16f(raw.x) + lo16f(a0), hi16f(raw.x) + hi16f(a0)), pack2(lo16f(raw.y) + lo16f(a1), hi16f(raw.y) + hi16f(a1)),
              pack2(lo16f(raw.z) + lo16f(a2), hi16f(raw.z) + hi16f(a2)), pack2(lo16f(raw.w) + lo16f(a3), hi16f(raw.w) + hi16f(a3))};
    *(v4u*)(hb + (size_t)row * DM + lane * 8) = hv;
    *(v4u*)(hn + (size_t)row * DM + lane * 8) = layernorm_vals(hv);
}

#define KX_DMA 0
#define XDELAY 0
#define YDELAY 0
constexpr int NX = 64;
constexpr int MEMX_STEP = 8192;
constexpr int PART_BUF = 64 * 64 * 128;
constexpr unsigned SPIN_CAP = 1u << 22;

#define STAMP(i) do {} while (0)
#define DIAG_DECL
#define DIAG_END
struct RecArgs {
    const u16* Wo;
    u16* K; u16* VT; u16* Q; u16* O;
    u16* KM; u16* VMT;
    unsigned char* memx; unsigned char* part;
    const u16* xn; const u16* Wq; const u16* Wk; const u16* Wv;
    unsigned* flags;
};
constexpr int NWORK = 128;
constexpr int FLAG_STRIDE = 16;

__device__ __forceinline__ void wait_flag(const unsigned* f) {
    unsigned spins = 0;
    while (__builtin_amdgcn_readfirstlane(__hip_atomic_load(f, __ATOMIC_RELAXED, __HIP_MEMORY_SCOPE_AGENT)) == 0u) {
        __builtin_amdgcn_s_sleep(2);
        if (++spins > SPIN_CAP) break;
    }
    asm volatile("" ::: "memory");
}

__device__ __forceinline__ void proj_tile(const RecArgs& a, unsigned char* lds, int rb, int c) {
    constexpr int BK = 64;
    unsigned char* sA = lds;
    unsigned char* sB = lds + 32768;
    const int tid = threadIdx.x, lane = tid & 63, wave = tid >> 6;
    const int wm = wave >> 1, wn = wave & 1;
    const int mat = c >> 5, head = c & 31;
    const u16* Bp = (mat == 0 ? a.Wk : (mat == 1 ? a.Wv : a.Wq)) + head * DH;
    const u16* Ap = a.xn + (size_t)rb * 128 * DM;
    const bool isv = (mat == 1);
    f32x4 acc[4][4];
#pragma unroll
    for (int i = 0; i < 4; ++i)
#pragma unroll
        for (int j = 0; j < 4; ++j) acc[i][j] = (f32x4){0.f, 0.f, 0.f, 0.f};
    v4u ra[4], rb_[4];
    auto gload = [&](int k0) {
#pragma unroll
        for (int i = 0; i < 4; ++i) { int q = tid + 256 * i; ra[i] = *(const v4u*)(Ap + (size_t)(q >> 3) * DM + k0 + (q & 7) * 8); }
#pragma unroll
        for (int i = 0; i < 4; ++i) { int q = tid + 256 * i; rb_[i] = *(const v4u*)(Bp + (size_t)(k0 + (q >> 4)) * HD + (q & 15) * 8); }
    };
    auto swrite = [&](int buf) {
#pragma unroll
        for (int i = 0; i < 4; ++i) { int q = tid + 256 * i; *(v4u*)(sA + buf * 16384 + offA(q >> 3, q & 7)) = ra[i]; }
#pragma unroll
        for (int i = 0; i < 4; ++i) { int q = tid + 256 * i; *(v4u*)(sB + buf * 16384 + offB(q >> 4, q & 15)) = rb_[i]; }
    };
    gload(0); swrite(0);
    __syncthreads();
    const int G = lane >> 4, l15 = lane & 15, tq = l15 >> 2, tp = lane & 3;
    for (int kt = 0; kt < DM / BK; ++kt) {
        const int buf = kt & 1;
        if (kt + 1 < DM / BK) gload((kt + 1) * BK);
#pragma unroll
        for (int ks = 0; ks < 2; ++ks) {
            bf16x8 af[4], bfr8[4];
#pragma unroll
            for (int i = 0; i < 4; ++i) af[i] = as8(*(const v4u*)(sA + buf * 16384 + offA(wm * 64 + i * 16 + l15, ks * 4 + G)));
#pragma unroll
            for (int j = 0; j < 4; ++j) {
                const int cc = wn * 4 + j;
                v2u lo = tr_read(sB + buf * 16384 + offB(32 * ks + 8 * G + tq, 2 * cc + (tp >> 1)) + 8 * (tp & 1));
                v2u hi = tr_read(sB + buf * 16384 + offB(32 * ks + 8 * G + 4 + tq, 2 * cc + (tp >> 1)) + 8 * (tp & 1));
                bfr8[j] = mk8(lo, hi);
            }
            if (isv) {
#pragma unroll
                for (int i = 0; i < 4; ++i)
#pragma unroll
                    for (int j = 0; j < 4; ++j) acc[i][j] = mfma16(af[i], bfr8[j], acc[i][j]);
            } else {
#pragma unroll
                for (int i = 0; i < 4; ++i)
#pragma unroll
                    for (int j = 0; j < 4; ++j) acc[i][j] = mfma16(bfr8[j], af[i], acc[i][j]);
            }
        }
        if (kt + 1 < DM / BK) swrite(buf ^ 1);
        __syncthreads();
    }
    unsigned char* img = lds;
#pragma unroll
    for (int i = 0; i < 4; ++i)
#pragma unroll
        for (int j = 0; j < 4; ++j) {
            v2u v = {pack2(acc[i][j][0], acc[i][j][1]), pack2(acc[i][j][2], acc[i][j][3])};
            if (isv) {
                const int e = (wn * 4 + j) * 16 + l15, row0 = wm * 64 + i * 16 + 4 * G;
                const int line = (row0 >> 5) * 128 + e;
                *(v2u*)(img + line * 64 + ((((row0 & 31) >> 2) ^ (e & 7)) * 8)) = v;
            } else {
                const int row = wm * 64 + i * 16 + l15, col0 = (wn * 4 + j) * 16 + 4 * G;
                *(v2u*)(img + row * 256 + (((col0 >> 3) ^ (row & 15)) * 16) + ((col0 & 4) * 2)) = v;
            }
        }
    __syncthreads();
    if (isv) {
        const __amdgpu_buffer_rsrc_t r = __builtin_amdgcn_make_buffer_rsrc(a.VT, 0, S_ * HD * 2, 0x00020000);
#pragma unroll
        for (int i = 0; i < 8; ++i) {
            const int q = tid + 256 * i;
            const int line = q >> 2, cq = q & 3, e = line & 127;
            v2u x0 = *(const v2u*)(img + line * 64 + (((2 * cq) ^ (e & 7)) * 8));
            v2u x1 = *(const v2u*)(img + line * 64 + (((2 * cq + 1) ^ (e & 7)) * 8));
            v4u v = {x0.x, x0.y, x1.x, x1.y};
            const int seg = rb * 4 + (line >> 7);
            __builtin_amdgcn_raw_buffer_store_b128(v, r, (unsigned)((((seg * NH + head) * DH + e) * 32 + cq * 8) * 2), 0, 16);
        }
    } else {
        const __amdgpu_buffer_rsrc_t r = __builtin_amdgcn_make_buffer_rsrc(mat == 0 ? a.K : a.Q, 0, S_ * HD * 2, 0x00020000);
#pragma unroll
        for (int i = 0; i < 8; ++i) {
            const int q = tid + 256 * i;
            const int row = q >> 4, ch = q & 15;
            v4u v = *(const v4u*)(img + row * 256 + ((ch ^ (row & 15)) * 16));
            __builtin_amdgcn_raw_buffer_store_b128(v, r, (unsigned)(((rb * 128 + row) * HD + head * DH + ch * 8) * 2), 0, 16);
        }
    }
    asm volatile("s_waitcnt vmcnt(0)" ::: "memory");
    __syncthreads();
    if (tid == 0) __hip_atomic_store(a.flags + (rb * 96 + c) * FLAG_STRIDE, 1u, __ATOMIC_RELAXED, __HIP_MEMORY_SCOPE_AGENT);
}

__device__ __forceinline__ void attn_unit(const RecArgs& a, int seg, int h, int lane) {
    const int G = lane >> 4, l15 = lane & 15;
    const float scale = 0.08838834764831845f;
    const __amdgpu_buffer_rsrc_t rq = __builtin_amdgcn_make_buffer_rsrc(a.Q, 0, S_ * HD * 2, 0x00020000);
    const __amdgpu_buffer_rsrc_t rk = __builtin_amdgcn_make_buffer_rsrc(a.K, 0, S_ * HD * 2, 0x00020000);
    const __amdgpu_buffer_rsrc_t rv = __builtin_amdgcn_make_buffer_rsrc(a.VT, 0, S_ * HD * 2, 0x00020000);
    const __amdgpu_buffer_rsrc_t rkm = __builtin_amdgcn_make_buffer_rsrc(a.KM, 0, NSEG * 8 * HD * 2, 0x00020000);
    const __amdgpu_buffer_rsrc_t rvm = __builtin_amdgcn_make_buffer_rsrc(a.VMT, 0, NSEG * 8 * HD * 2, 0x00020000);
    const int rb = seg >> 2;
    wait_flag(a.flags + (rb * 96 + h) * FLAG_STRIDE);
    wait_flag(a.flags + (rb * 96 + 32 + h) * FLAG_STRIDE);
    wait_flag(a.flags + (rb * 96 + 64 + h) * FLAG_STRIDE);
    v4u k1[4], k2[4], q0[4], q1[4];
    v2u v1[8], v2[8];
#pragma unroll
    for (int ks = 0; ks < 4; ++ks) {
        const unsigned eo = (unsigned)(h * DH + ks * 32 + 8 * G) * 2;
        k1[ks] = __builtin_amdgcn_raw_buffer_load_b128(rk, (unsigned)(seg * 32 + l15) * (HD * 2) + eo, 0, 16);
        k2[ks] = __builtin_amdgcn_raw_buffer_load_b128(rk, (unsigned)(seg * 32 + 16 + l15) * (HD * 2) + eo, 0, 16);
        q0[ks] = __builtin_amdgcn_raw_buffer_load_b128(rq, (unsigned)(seg * 32 + l15) * (HD * 2) + eo, 0, 16);
        q1[ks] = __builtin_amdgcn_raw_buffer_load_b128(rq, (unsigned)(seg * 32 + 16 + l15) * (HD * 2) + eo, 0, 16);
    }
#pragma unroll
    for (int et = 0; et < 8; ++et) {
        const unsigned vo = (unsigned)(((seg * NH + h) * DH + et * 16 + l15) * 32 + 4 * G) * 2;
        v1[et] = __builtin_amdgcn_raw_buffer_load_b64(rv, vo, 0, 16);
        v2[et] = __builtin_amdgcn_raw_buffer_load_b64(rv, vo + 32, 0, 16);
    }
    v4u k0[4]; v2u vm[8];
    {
        unsigned spins = 0;
        const unsigned* probe = (const unsigned*)(a.VMT + ((size_t)(seg * NH + h) * DH + 127) * 8);
        while (__builtin_amdgcn_readfirstlane(__hip_atomic_load(probe, __ATOMIC_RELAXED, __HIP_MEMORY_SCOPE_AGENT)) == 0xFFFFFFFFu) {
            __builtin_amdgcn_s_sleep(100);
            if (++spins > SPIN_CAP) break;
        }
        for (;;) {
            unsigned mx = 0;
#pragma unroll
            for (int ks = 0; ks < 4; ++ks) {
                k0[ks] = __builtin_amdgcn_raw_buffer_load_b128(rkm, (unsigned)((seg * 8 + (l15 & 7)) * HD + h * DH + ks * 32 + 8 * G) * 2, 0, 16);
            }
#pragma unroll
            for (int et = 0; et < 8; ++et) vm[et] = __builtin_amdgcn_raw_buffer_load_b64(rvm, (unsigned)(((seg * NH + h) * DH + et * 16 + l15) * 8 + 4 * (G & 1)) * 2, 0, 16);
#pragma unroll
            for (int ks = 0; ks < 4; ++ks) mx = max(mx, umax4(k0[ks]));
#pragma unroll
            for (int et = 0; et < 8; ++et) mx = umax3(mx, vm[et].x, vm[et].y);
            asm volatile("" ::: "memory");
            if (__all(mx != 0xFFFFFFFFu)) break;
            __builtin_amdgcn_s_sleep(16);
            if (++spins > SPIN_CAP) break;
        }
    }
    f32x4 st[3][2];
#pragma unroll
    for (int i = 0; i < 3; ++i) { st[i][0] = (f32x4){0.f, 0.f, 0.f, 0.f}; st[i][1] = st[i][0]; }
#pragma unroll
    for (int ks = 0; ks < 4; ++ks) {
        st[0][0] = mfma16(as8(k0[ks]), as8(q0[ks]), st[0][0]); st[0][1] = mfma16(as8(k0[ks]), as8(q1[ks]), st[0][1]);
        st[1][0] = mfma16(as8(k1[ks]), as8(q0[ks]), st[1][0]); st[1][1] = mfma16(as8(k1[ks]), as8(q1[ks]), st[1][1]);
        st[2][0] = mfma16(as8(k2[ks]), as8(q0[ks]), st[2][0]); st[2][1] = mfma16(as8(k2[ks]), as8(q1[ks]), st[2][1]);
    }
    bf16x8 pf[2][2];
#pragma unroll
    for (int qt = 0; qt < 2; ++qt) {
        const int t = qt * 16 + l15;
        float v[3][4];
        float m = -3.0e38f;
#pragma unroll
        for (int i = 0; i < 3; ++i)
#pragma unroll
            for (int r = 0; r < 4; ++r) {
                float sc = bfr(bfr(st[i][qt][r]) * scale);
                bool ok;
                if (i == 0) ok = (G < 2); else ok = ((i - 1) * 16 + 4 * G + r) <= t;
                bool exist = (i != 0) || (G < 2);
                sc = ok ? sc : bfr(-1.0e9f);
                v[i][r] = exist ? sc : -3.0e38f;
                m = fmaxf(m, v[i][r]);
            }
        m = xrow16_max(m);
        float sum = 0.f;
#pragma unroll
        for (int i = 0; i < 3; ++i)
#pragma unroll
            for (int r = 0; r < 4; ++r) { bool exist = (i != 0) || (G < 2); v[i][r] = exist ? __expf(v[i][r] - m) : 0.f; sum += v[i][r]; }
        sum = xrow16_sum(sum);
        const float inv = 1.0f / sum;
        v4u p0 = {pack2(v[0][0] * inv, v[0][1] * inv), pack2(v[0][2] * inv, v[0][3] * inv), pack2(v[1][0] * inv, v[1][1] * inv), pack2(v[1][2] * inv, v[1][3] * inv)};
        v4u p1 = {pack2(v[2][0] * inv, v[2][1] * inv), pack2(v[2][2] * inv, v[2][3] * inv), 0u, 0u};
        pf[qt][0] = as8(p0); pf[qt][1] = as8(p1);
    }
#pragma unroll
    for (int et = 0; et < 8; ++et) {
        bf16x8 a0 = mk8(vm[et], v1[et]);
        bf16x8 a1 = mk8(v2[et], (v2u){0u, 0u});
#pragma unroll
        for (int qt = 0; qt < 2; ++qt) {
            f32x4 o = (f32x4){0.f, 0.f, 0.f, 0.f};
            o = mfma16(a0, pf[qt][0], o);
            o = mfma16(a1, pf[qt][1], o);
            v2u ov = {pack2(o[0], o[1]), pack2(o[2], o[3])};
            *(v2u*)(a.O + ((size_t)seg * 32 + qt * 16 + l15) * HD + h * DH + et * 16 + 4 * G) = ov;
        }
    }
}

constexpr int L_WO = 0;
constexpr int L_MEM = 65536;
constexpr int L_T = L_MEM + 8192;
constexpr int L_VM = L_T + 4096;
constexpr int L_KX = L_VM + 2048;
constexpr int L_VX = L_KX + 8192;
constexpr int L_STG = 96 * 1024;
constexpr int L_TOTAL = 160 * 1024;

__global__ void __launch_bounds__(256, 1) k_rec(RecArgs a) {
    extern __shared__ __attribute__((aligned(16))) unsigned char lds[];
    const int tid = threadIdx.x, lane = tid & 63;
    const int wave = __builtin_amdgcn_readfirstlane(tid >> 6);
    const int G = lane >> 4, l15 = lane & 15;
    const __amdgpu_buffer_rsrc_t rmem = __builtin_amdgcn_make_buffer_rsrc(a.memx, 0, (NSEG + 1) * MEMX_STEP, 0x00020000);
    const __amdgpu_buffer_rsrc_t rpart = __builtin_amdgcn_make_buffer_rsrc(a.part, 0, 2 * PART_BUF, 0x00020000);

    if (blockIdx.x >= 2 * NX) {
        const int w = blockIdx.x - 2 * NX;
        for (int i = w; i < 16 * 96; i += NWORK) proj_tile(a, lds, i / 96, i % 96);
        for (int u = w; u < NSEG * 8; u += NWORK) attn_unit(a, u >> 3, (u & 7) * 4 + wave, lane);
        return;
    }
    if (blockIdx.x >= NX) {
        if (wave != 0) return;
        const int y = blockIdx.x - NX;
        DIAG_DECL
        unsigned long long typub = 0;
        bf16x8 sel;
#pragma unroll
        for (int j = 0; j < 8; ++j) sel[j] = (l15 == j) ? (__bf16)1.0f : (__bf16)0.0f;
        for (int s = 0; s < NSEG; ++s) {
            const unsigned base = (s & 1) * PART_BUF + y * 8192 + lane * 16;
            v4u m[8];
            unsigned spins = 0;
            for (;;) {
#pragma unroll
                for (int t = 0; t < 8; ++t) m[t] = __builtin_amdgcn_raw_buffer_load_b128(rpart, base + t * 1024, 0, 16);
                unsigned mx = 0;
#pragma unroll
                for (int t = 0; t < 8; ++t) mx = umax3(mx, umax3(m[t].x, m[t].y, m[t].z), m[t].w);
                asm volatile("" ::: "memory");
                if (__all(mx != 0xFFFFFFFFu)) break;
                if (++spins > SPIN_CAP) break;
            }
            STAMP(0);
            f32x4 acc0 = (f32x4){0.f, 0.f, 0.f, 0.f}, acc1 = acc0;
#pragma unroll
            for (int t = 0; t < 8; t += 2) { acc0 = mfma16(sel, as8(m[t]), acc0); acc1 = mfma16(sel, as8(m[t + 1]), acc1); }
            acc0 += acc1;
#pragma unroll
            for (int r = 0; r < 4; ++r) acc0[r] += dppf<0x128>(acc0[r]);
            if (lane < 32 && l15 < 8) {
                v2u v = {nosent(pack2(acc0[0], acc0[1])), nosent(pack2(acc0[2], acc0[3]))};
                __builtin_amdgcn_raw_buffer_store_b64(v, rmem, (s + 1) * MEMX_STEP + y * 128 + l15 * 16 + G * 8, 0, 16);
            }
            STAMP(1);
            const v4u sent = {0xFFFFFFFFu, 0xFFFFFFFFu, 0xFFFFFFFFu, 0xFFFFFFFFu};
#pragma unroll
            for (int t = 0; t < 8; ++t) __builtin_amdgcn_raw_buffer_store_b128(sent, rpart, base + t * 1024, 0, 16);
            asm volatile("s_waitcnt vmcnt(0)" ::: "memory");
        }
        DIAG_END;
        return;
    }

    const int b = blockIdx.x, h = b >> 1, g = b & 1;
    bf16x16 wqk[2][16];
    bf16x8 wv[16];
    {
        const int tq_ = l15 >> 2, tp_ = lane & 3;
        const unsigned stg = __builtin_amdgcn_readfirstlane(lds_addr(lds)) + L_STG + wave * 1024;
        int srow[4], sch[4];
#pragma unroll
        for (int i = 0; i < 4; ++i) { const int q = tid + 256 * i; srow[i] = q >> 4; sch[i] = ((q & 15) ^ (((srow[i] & 3) << 2) | ((srow[i] >> 2) & 3))) * 8; }
        auto issue = [&](int c) {
            const u16* s0; const u16* s1; int ld;
            if (c < 8) { s0 = a.Wq + (size_t)(64 * c) * HD + h * DH; s1 = a.Wk + (size_t)(64 * c) * HD + h * DH; ld = HD; }
            else if (c < 12) { s0 = a.Wv + (size_t)(128 * (c - 8)) * HD + h * DH; s1 = s0 + (size_t)64 * HD; ld = HD; }
            else { s0 = a.Wo + (size_t)(h * DH + 64 * g) * DM + 256 * (c - 12); s1 = s0 + 128; ld = DM; }
            const unsigned dst = stg + (c & 1) * 32768;
#pragma unroll
            for (int i = 0; i < 4; ++i) glds16(s0 + (size_t)srow[i] * ld + sch[i], dst + i * 4096);
#pragma unroll
            for (int i = 0; i < 4; ++i) glds16(s1 + (size_t)srow[i] * ld + sch[i], dst + 16384 + i * 4096);
        };
        issue(0);
#pragma unroll
        for (int c = 0; c < 14; ++c) {
            if (c + 1 < 14) { issue(c + 1); asm volatile("s_waitcnt vmcnt(8)" ::: "memory"); }
            else asm volatile("s_waitcnt vmcnt(0)" ::: "memory");
            __builtin_amdgcn_s_barrier();
            const unsigned char* t0 = lds + L_STG + (c & 1) * 32768;
            const unsigned char* t1 = t0 + 16384;
            if (c < 8) {
#pragma unroll
                for (int tt = 0; tt < 2; ++tt)
#pragma unroll
                    for (int u = 0; u < 2; ++u) {
                        v4u hf[2];
#pragma unroll
                        for (int half = 0; half < 2; ++half) {
                            const unsigned off = offB(32 * tt + 16 * half + 4 * G + tq_, 2 * (2 * wave + u) + (tp_ >> 1)) + 8 * (tp_ & 1);
                            v2u q = tr_read(t0 + off), k = tr_read(t1 + off);
                            hf[half] = (v4u){q.x, k.x, q.y, k.y};
                        }
                        bf16x8 lo = as8(hf[0]), hi = as8(hf[1]);
#pragma unroll
                        for (int j = 0; j < 8; ++j) { wqk[u][2 * c + tt][j] = lo[j]; wqk[u][2 * c + tt][8 + j] = hi[j]; }
                    }
            } else if (c < 12) {
#pragma unroll
                for (int j = 0; j < 2; ++j)
#pragma unroll
                    for (int tt = 0; tt < 2; ++tt) {
                        const unsigned char* tb = j ? t1 : t0;
                        v2u lo = tr_read(tb + offB(32 * tt + 8 * G + tq_, 2 * (4 * g + wave) + (tp_ >> 1)) + 8 * (tp_ & 1));
                        v2u hi = tr_read(tb + offB(32 * tt + 8 * G + 4 + tq_, 2 * (4 * g + wave) + (tp_ >> 1)) + 8 * (tp_ & 1));
                        wv[4 * (c - 8) + 2 * j + tt] = mk8(lo, hi);
                    }
            } else {
#pragma unroll
                for (int j = 0; j < 2; ++j)
#pragma unroll
                    for (int kk = 0; kk < 2; ++kk)
#pragma unroll
                        for (int half = 0; half < 2; ++half) {
                            const unsigned char* tb = j ? t1 : t0;
                            const int e0 = 32 * kk + 16 * (G & 1) + 4 * (2 * half + (G >> 1)) + tq_;
                            v2u A = tr_read(tb + offB(e0, 4 * wave + (tp_ >> 1)) + 8 * (tp_ & 1));
                            v2u B = tr_read(tb + offB(e0, 4 * wave + 2 + (tp_ >> 1)) + 8 * (tp_ & 1));
                            const int dp = 8 * (c - 12) + 4 * j + wave;
                            *(v4u*)(lds + L_WO + ((dp * 2 + kk) * 2 + half) * 1024 + lane * 16) = (v4u){A.x, B.x, A.y, B.y};
                        }
            }
            asm volatile("s_waitcnt lgkmcnt(0)" ::: "memory");
            __builtin_amdgcn_s_barrier();
        }
    }
    const int sidx = (lane & 8) ? 0xEEEE : 0x4444;
    const int tq = l15 >> 2, tp = lane & 3;
    const unsigned char* memrd = lds + L_MEM + (8 * G + tq) * 16 + (tp & 1) * 8;
    const unsigned char* trd = lds + L_T + (8 * G + tq) * 32 + (tp & 1) * 8;
    const unsigned char* wp = lds + L_WO + wave * 16384 + lane * 16;
    const float sl2e = 0.08838834764831845f * 1.4426950408889634f;
    const bool exist0 = (G < 2);
    constexpr int PD = 4;
    const __amdgpu_buffer_rsrc_t rk = __builtin_amdgcn_make_buffer_rsrc(a.K, 0, S_ * HD * 2, 0x00020000);
    const __amdgpu_buffer_rsrc_t rv = __builtin_amdgcn_make_buffer_rsrc(a.VT, 0, S_ * HD * 2, 0x00020000);
    const __amdgpu_buffer_rsrc_t rkm = __builtin_amdgcn_make_buffer_rsrc(a.KM, 0, NSEG * 8 * HD * 2, 0x00020000);
    const __amdgpu_buffer_rsrc_t rvm = __builtin_amdgcn_make_buffer_rsrc(a.VMT, 0, NSEG * 8 * HD * 2, 0x00020000);

    wait_flag(a.flags + h * FLAG_STRIDE);
    wait_flag(a.flags + (32 + h) * FLAG_STRIDE);
    const unsigned lds0 = __builtin_amdgcn_readfirstlane(lds_addr(lds));
    const u16* ksrc0; const u16* ksrc1; const u16* vsrc;
    {
        const int p0 = tid, p1 = tid + 256;
        ksrc0 = a.K + (size_t)(p0 >> 4) * HD + h * DH + (((p0 & 15) ^ ((p0 >> 4) & 15)) * 8);
        ksrc1 = a.K + (size_t)(p1 >> 4) * HD + h * DH + (((p1 & 15) ^ ((p1 >> 4) & 15)) * 8);
        vsrc = a.VT + ((size_t)h * DH + 64 * g) * 32 + tid * 8;
    }
    auto load_kv = [&](int s) {
        glds16_sc1(ksrc0 + (size_t)s * 32 * HD, lds0 + L_KX + wave * 1024);
        glds16_sc1(ksrc1 + (size_t)s * 32 * HD, lds0 + L_KX + 4096 + wave * 1024);
        glds16_sc1(vsrc + (size_t)s * NH * DH * 32, lds0 + L_VX + wave * 1024);
    };
    v4u kx[2][4]; v2u vx[2];
    auto load_kx = [&](int s) {
#pragma unroll
        for (int kt = 0; kt < 2; ++kt)
#pragma unroll
            for (int ks = 0; ks < 4; ++ks) kx[kt][ks] = __builtin_amdgcn_raw_buffer_load_b128(rk, (unsigned)((s * 32 + kt * 16 + l15) * HD + h * DH + ks * 32 + 8 * G) * 2, 0, 16);
        const unsigned vo = (unsigned)(((s * NH + h) * DH + 64 * g + wave * 16 + l15) * 32 + 4 * G) * 2;
        vx[0] = __builtin_amdgcn_raw_buffer_load_b64(rv, vo, 0, 16); vx[1] = __builtin_amdgcn_raw_buffer_load_b64(rv, vo + 32, 0, 16);
    };
    load_kx(0);
    unsigned long long tpub = 0;
    DIAG_DECL
    for (int s = 0; s < NSEG; ++s) {
        const int sn = (s + 1 < NSEG) ? s + 1 : s;
        {
            v4u m0, m1;
            unsigned spins = 0;
            for (;;) {
                m0 = __builtin_amdgcn_raw_buffer_load_b128(rmem, s * MEMX_STEP + tid * 16, 0, 16);
                m1 = __builtin_amdgcn_raw_buffer_load_b128(rmem, s * MEMX_STEP + 4096 + tid * 16, 0, 16);
                unsigned mx = umax3(umax3(m0.x, m0.y, m0.z), umax3(m0.w, m1.x, m1.y), max(m1.z, m1.w));
                asm volatile("" ::: "memory");
                if (__all(mx != 0xFFFFFFFFu)) break;
                if (++spins > SPIN_CAP) break;
            }
            *(v4u*)(lds + L_MEM + tid * 16) = m0;
            *(v4u*)(lds + L_MEM + 4096 + tid * 16) = m1;
            asm volatile("s_waitcnt vmcnt(0)" ::: "memory");
        }
        STAMP(0);
        __syncthreads();
        STAMP(1);
        v2u lo[16], hi[16];
        f32x4 acc0 = (f32x4){0.f, 0.f, 0.f, 0.f}, acc1 = acc0;
#pragma unroll
        for (int t = 0; t < PD; ++t) { lo[t] = tr_read(memrd + t * 512); hi[t] = tr_read(memrd + t * 512 + 64); }
        SB0();
#pragma unroll
        for (int t = 0; t < 16; ++t) {
            if (t + PD < 16) { lo[t + PD] = tr_read(memrd + (t + PD) * 512); hi[t + PD] = tr_read(memrd + (t + PD) * 512 + 64); }
            acc0 = __builtin_amdgcn_smfmac_f32_16x16x64_bf16(mk8(lo[t], hi[t]), wqk[0][t], acc0, sidx, 0, 0);
            acc1 = __builtin_amdgcn_smfmac_f32_16x16x64_bf16(mk8(lo[t], hi[t]), wqk[1][t], acc1, sidx, 0, 0);
            SB0();
        }
        {
            v2u v0 = {pack2(acc0[0], acc0[1]), pack2(acc0[2], acc0[3])};
            v2u v1 = {pack2(acc1[0], acc1[1]), pack2(acc1[2], acc1[3])};
            *(v2u*)(lds + L_T + (wave * 32 + l15) * 32 + G * 8) = v0;
            *(v2u*)(lds + L_T + (wave * 32 + 16 + l15) * 32 + G * 8) = v1;
        }
        STAMP(2);
        __syncthreads();
        STAMP(3);
        v2u q_lo[4], q_hi[4], k_lo[4], k_hi[4];
#pragma unroll
        for (int ks = 0; ks < 4; ++ks) {
            q_lo[ks] = tr_read(trd + ks * 1024); q_hi[ks] = tr_read(trd + ks * 1024 + 128);
            k_lo[ks] = tr_read(trd + ks * 1024 + 16); k_hi[ks] = tr_read(trd + ks * 1024 + 128 + 16);
        }
        f32x4 accv = (f32x4){0.f, 0.f, 0.f, 0.f};
#pragma unroll
        for (int t = 0; t < 4; ++t) accv = mfma16(mk8(lo[t], hi[t]), wv[t], accv);
        SB0();
        f32x4 st[3];
#pragma unroll
        for (int i = 0; i < 3; ++i) st[i] = (f32x4){0.f, 0.f, 0.f, 0.f};
#pragma unroll
        for (int ks = 0; ks < 4; ++ks) {
            bf16x8 qf = mk8(q_lo[ks], q_hi[ks]);
            st[1] = mfma16(as8(kx[0][ks]), qf, st[1]);
            st[2] = mfma16(as8(kx[1][ks]), qf, st[2]);
            st[0] = mfma16(mk8(k_lo[ks], k_hi[ks]), qf, st[0]);
        }
        SB0();
#pragma unroll
        for (int t = 4; t < 16; ++t) accv = mfma16(mk8(lo[t], hi[t]), wv[t], accv);
        v4u wo[4][2][2];
#pragma unroll
        for (int i = 0; i < 4; ++i) { wo[i][0][0] = *(const v4u*)(wp + i * 4096); wo[i][0][1] = *(const v4u*)(wp + i * 4096 + 1024); }
        float m = fmaxf(fmaxf(fmaxf(st[1][0], st[1][1]), fmaxf(st[1][2], st[1][3])), fmaxf(fmaxf(st[2][0], st[2][1]), fmaxf(st[2][2], st[2][3])));
        float m0 = fmaxf(fmaxf(st[0][0], st[0][1]), fmaxf(st[0][2], st[0][3]));
        m = exist0 ? fmaxf(m, m0) : m;
        m = xrow16_max(m);
        const float nm = -m * sl2e;
        float v[3][4];
#pragma unroll
        for (int i = 0; i < 3; ++i)
#pragma unroll
            for (int r = 0; r < 4; ++r) v[i][r] = __builtin_amdgcn_exp2f(__builtin_fmaf(st[i][r], sl2e, nm));
#pragma unroll
        for (int r = 0; r < 4; ++r) v[0][r] = exist0 ? v[0][r] : 0.f;
        float sum = ((v[0][0] + v[0][1]) + (v[0][2] + v[0][3])) + ((v[1][0] + v[1][1]) + (v[1][2] + v[1][3])) + ((v[2][0] + v[2][1]) + (v[2][2] + v[2][3]));
        sum = xrow16_sum(sum);
        const float inv = __builtin_amdgcn_rcpf(sum);
        v4u p0 = {pack2(v[0][0] * inv, v[0][1] * inv), pack2(v[0][2] * inv, v[0][3] * inv), pack2(v[1][0] * inv, v[1][1] * inv), pack2(v[1][2] * inv, v[1][3] * inv)};
        v4u p1 = {pack2(v[2][0] * inv, v[2][1] * inv), pack2(v[2][2] * inv, v[2][3] * inv), 0u, 0u};
        v2u vm = {pack2(accv[0], accv[1]), pack2(accv[2], accv[3])};
        f32x4 o = mfma16(mk8(vx[1], (v2u){0u, 0u}), as8(p1), (f32x4){0.f, 0.f, 0.f, 0.f});
        o = mfma16(mk8(vm, vx[0]), as8(p0), o);
        *(v2u*)(lds + L_VM + wave * 512 + lane * 8) = (v2u){pack2(o[0], o[1]), pack2(o[2], o[3])};
        STAMP(4);
        __syncthreads();
        STAMP(5);
        v2u ob[4];
#pragma unroll
        for (int et = 0; et < 4; ++et) ob[et] = *(const v2u*)(lds + L_VM + et * 512 + lane * 8);
#pragma unroll
        for (int i = 0; i < 4; ++i) { wo[i][1][0] = *(const v4u*)(wp + i * 4096 + 2048); wo[i][1][1] = *(const v4u*)(wp + i * 4096 + 3072); }
        SB0();
        const bf16x8 oa0 = mk8(ob[0], ob[1]), oa1 = mk8(ob[2], ob[3]);
        f32x4 d[4];
#pragma unroll
        for (int kk = 0; kk < 2; ++kk)
#pragma unroll
            for (int i = 0; i < 4; ++i) {
                bf16x16 wf;
                bf16x8 w0b = as8(wo[i][kk][0]), w1b = as8(wo[i][kk][1]);
#pragma unroll
                for (int j = 0; j < 8; ++j) { wf[j] = w0b[j]; wf[8 + j] = w1b[j]; }
                d[i] = __builtin_amdgcn_smfmac_f32_16x16x64_bf16(kk == 0 ? oa0 : oa1, wf, kk == 0 ? (f32x4){0.f, 0.f, 0.f, 0.f} : d[i], sidx, 0, 0);
            }
#pragma unroll
        for (int i = 0; i < 4; ++i) {
            const int dcol = 32 * (wave * 4 + i) + 16 * (G >> 1) + l15;
            v2u pv = {nosent(pack2(d[i][0], d[i][1])), nosent(pack2(d[i][2], d[i][3]))};
            __builtin_amdgcn_raw_buffer_store_b64(pv, rpart, (s & 1) * PART_BUF + (dcol >> 3) * 8192 + b * 128 + (dcol & 7) * 16 + (G & 1) * 8, 0, 16);
        }
        STAMP(6);
        if (G < 2) {
            v2u vs = {nosent(vm.x), nosent(vm.y)};
            __builtin_amdgcn_raw_buffer_store_b64(vs, rvm, (unsigned)(((s * NH + h) * DH + 64 * g + wave * 16 + l15) * 8 + 4 * G) * 2, 0, 16);
        }
        if (g == 0 && l15 < 8) {
            v2u kl = wave == 0 ? k_lo[0] : (wave == 1 ? k_lo[1] : (wave == 2 ? k_lo[2] : k_lo[3]));
            v2u kh = wave == 0 ? k_hi[0] : (wave == 1 ? k_hi[1] : (wave == 2 ? k_hi[2] : k_hi[3]));
            v4u kv = {nosent(kl.x), nosent(kl.y), nosent(kh.x), nosent(kh.y)};
            __builtin_amdgcn_raw_buffer_store_b128(kv, rkm, (unsigned)((s * 8 + l15) * HD + h * DH + wave * 32 + 8 * G) * 2, 0, 16);
        }
        SB0();
        load_kx(sn);
        if ((s & 3) == 2 && s + 2 < NSEG) {
            wait_flag(a.flags + (((s + 2) >> 2) * 96 + h) * FLAG_STRIDE);
            wait_flag(a.flags + (((s + 2) >> 2) * 96 + 32 + h) * FLAG_STRIDE);
        }
    }
    DIAG_END;
}

extern "C" void kernel_launch(void* const* d_in, const int* in_sizes, int n_in, void* d_out, int out_size, void* d_ws, size_t ws_size,
                              hipStream_t stream) {
    const u16* x = (const u16*)d_in[0];
    const u16* mem0 = (const u16*)d_in[1];
    const u16* Wq = (const u16*)d_in[2];
    const u16* Wk = (const u16*)d_in[3];
    const u16* Wv = (const u16*)d_in[4];
    const u16* Wo = (const u16*)d_in[5];
    const u16* Wg = (const u16*)d_in[8];
    const u16* Wu = (const u16*)d_in[9];
    const u16* Wd = (const u16*)d_in[10];
    u16* out = (u16*)d_out;

    unsigned char* ws = (unsigned char*)d_ws;
    size_t off = 0;
    auto alloc = [&](size_t bytes) { unsigned char* p = ws + off; off += (bytes + 255) & ~(size_t)255; return p; };
    constexpr size_t KM_BYTES = (size_t)NSEG * 8 * HD * 2;
    constexpr size_t SENT_BYTES = (size_t)2 * PART_BUF + (size_t)(NSEG + 1) * MEMX_STEP + 2 * KM_BYTES;
    unsigned* flags = (unsigned*)alloc(16 * 96 * FLAG_STRIDE * 4);
    unsigned char* xchg = alloc(SENT_BYTES);
    unsigned char* part = xchg;
    unsigned char* memx = xchg + 2 * PART_BUF;
    u16* KM = (u16*)(memx + (size_t)(NSEG + 1) * MEMX_STEP);
    u16* VMT = (u16*)((unsigned char*)KM + KM_BYTES);
    u16* xn = (u16*)alloc((size_t)S_ * DM * 2);
    u16* Q = (u16*)alloc((size_t)S_ * HD * 2);
    u16* K = (u16*)alloc((size_t)S_ * HD * 2);
    u16* VT = (u16*)alloc((size_t)S_ * HD * 2);
    u16* O = (u16*)alloc((size_t)S_ * HD * 2);
    u16* hb = (u16*)alloc((size_t)S_ * DM * 2);
    u16* hn = (u16*)alloc((size_t)S_ * DM * 2);
    u16* act = (u16*)alloc((size_t)S_ * FF * 2);
    float* Pk = (float*)alloc((size_t)2 * S_ * DM * 4);

    k_prep<<<S_ / 4, 256, 0, stream>>>(x, xn, mem0, (u16*)memx, (v4u*)xchg, (int)(SENT_BYTES / 16), (int)(2 * PART_BUF / 16), (v4u*)flags, 16 * 96 * FLAG_STRIDE / 4);
    {
        static bool attr_set = false;
        if (!attr_set) { (void)hipFuncSetAttribute((const void*)k_rec, hipFuncAttributeMaxDynamicSharedMemorySize, L_TOTAL); attr_set = true; }
        RecArgs r{Wo, K, VT, Q, O, KM, VMT, memx, part, xn, Wq, Wk, Wv, flags};
        k_rec<<<2 * NX + NWORK, 256, L_TOTAL, stream>>>(r);
    }
    {
        static bool attr2 = false;
        if (!attr2) {
            (void)hipFuncSetAttribute((const void*)k_gemm2<1, 128, 1, 6>, hipFuncAttributeMaxDynamicSharedMemorySize, 6 * (128 * 128 + 8192));
            (void)hipFuncSetAttribute((const void*)k_gemm2<2, 128, 2, 2>, hipFuncAttributeMaxDynamicSharedMemorySize, 2 * (128 * 128 + 16384));
            (void)hipFuncSetAttribute((const void*)k_gemm2<3, 64, 1, 8>, hipFuncAttributeMaxDynamicSharedMemorySize, 8 * (64 * 128 + 8192));
            attr2 = true;
        }
        GemmArgs g1{O, Wo, nullptr, nullptr, nullptr, (u16*)Pk, nullptr, nullptr, HD, DM, HD / 2, (DM / 64) | (2 << 16)};
        k_gemm2<1, 128, 1, 6><<<(DM / 64) * (S_ / 128) * 2, 256, 6 * (128 * 128 + 8192), stream>>>(g1);
        k_res_ln<2><<<S_ / 4, 256, 0, stream>>>(Pk, x, hb, hn);
        GemmArgs g2{hn, Wg, Wu, nullptr, nullptr, act, nullptr, nullptr, DM, FF, DM, (FF / 64) | (1 << 16)};
        k_gemm2<2, 128, 2, 2><<<(FF / 64) * (S_ / 128), 256, 2 * (128 * 128 + 16384), stream>>>(g2);
        GemmArgs g3{act, Wd, nullptr, nullptr, hb, out, nullptr, nullptr, FF, DM, FF, (DM / 64) | (1 << 16)};
        k_gemm2<3, 64, 1, 8><<<(DM / 64) * (S_ / 64), 256, 8 * (64 * 128 + 8192), stream>>>(g3);
    }
}
```

```cpp
#include <hip/hip_runtime.h>
#include <hip/hip_bf16.h>

typedef unsigned short u16;
typedef __bf16 bf16x8 __attribute__((ext_vector_type(8)));
typedef __bf16 bf16x16 __attribute__((ext_vector_type(16)));
typedef float f32x4 __attribute__((ext_vector_type(4)));
typedef unsigned v4u __attribute__((ext_vector_type(4)));
typedef unsigned v2u __attribute__((ext_vector_type(2)));
typedef short v4s __attribute__((ext_vector_type(4)));
#define LAS __attribute__((address_space(3)))

constexpr int S_ = 2048, DM = 512, NH = 32, DH = 128, HD = NH * DH, MEMN = 8, SEG = 32, NSEG = 64, FF = 1792;

__device__ __forceinline__ float bf2f(u16 v) { return __uint_as_float(((unsigned)v) << 16); }
__device__ __forceinline__ u16 f2bf(float f) { return __builtin_bit_cast(u16, (__bf16)f); }
__device__ __forceinline__ float bfr(float f) { return bf2f(f2bf(f)); }
typedef float f32x2 __attribute__((ext_vector_type(2)));
typedef __bf16 bf16x2 __attribute__((ext_vector_type(2)));
__device__ __forceinline__ unsigned pack2(float a, float b) { f32x2 t = {a, b}; return __builtin_bit_cast(unsigned, __builtin_convertvector(t, bf16x2)); }
__device__ __forceinline__ float lo16f(unsigned w) { return __uint_as_float(w << 16); }
__device__ __forceinline__ float hi16f(unsigned w) { return __uint_as_float(w & 0xFFFF0000u); }
template <int CTRL> __device__ __forceinline__ float dppf(float x) {
    return __builtin_bit_cast(float, __builtin_amdgcn_mov_dpp(__builtin_bit_cast(int, x), CTRL, 0xf, 0xf, true));
}
__device__ __forceinline__ float xrow16_max(float x) {
    auto s = __builtin_amdgcn_permlane16_swap(__float_as_uint(x), __float_as_uint(x), false, false);
    x = fmaxf(__uint_as_float(s[0]), __uint_as_float(s[1]));
    auto t = __builtin_amdgcn_permlane32_swap(__float_as_uint(x), __float_as_uint(x), false, false);
    return fmaxf(__uint_as_float(t[0]), __uint_as_float(t[1]));
}
__device__ __forceinline__ float xrow16_sum(float x) {
    auto s = __builtin_amdgcn_permlane16_swap(__float_as_uint(x), __float_as_uint(x), false, false);
    x = __uint_as_float(s[0]) + __uint_as_float(s[1]);
    auto t = __builtin_amdgcn_permlane32_swap(__float_as_uint(x), __float_as_uint(x), false, false);
    return __uint_as_float(t[0]) + __uint_as_float(t[1]);
}
__device__ __forceinline__ unsigned umax3(unsigned a, unsigned b, unsigned c) { return max(max(a, b), c); }
__device__ __forceinline__ unsigned umax4(v4u v) { return max(umax3(v.x, v.y, v.z), v.w); }
__device__ __forceinline__ bool valid16(v4u v) { return umax4(v) != 0xFFFFFFFFu; }
#define SB0() __builtin_amdgcn_sched_barrier(0)
__device__ __forceinline__ void glds16(const void* gsrc, unsigned lds_dst) {
    unsigned keep;
    asm volatile("s_mov_b32 %0, m0\n\ts_mov_b32 m0, %2\n\ts_nop 0\n\tglobal_load_lds_dwordx4 %1, off\n\ts_mov_b32 m0, %0" : "=&s"(keep) : "v"(gsrc), "s"(lds_dst) : "memory");
}
__device__ __forceinline__ void glds16_sc1(const void* gsrc, unsigned lds_dst) {
    unsigned keep;
    asm volatile("s_mov_b32 %0, m0\n\ts_mov_b32 m0, %2\n\ts_nop 0\n\tglobal_load_lds_dwordx4 %1, off sc1\n\ts_mov_b32 m0, %0" : "=&s"(keep) : "v"(gsrc), "s"(lds_dst) : "memory");
}
__device__ __forceinline__ unsigned lds_addr(const void* p) { return (unsigned)(size_t)(LAS const unsigned char*)p; }
__device__ __forceinline__ unsigned nosent(unsigned w) { return min(w, 0xFFFFFFFEu); }
__device__ __forceinline__ v2u tr_read(const void* p) {
    return __builtin_bit_cast(v2u, __builtin_amdgcn_ds_read_tr16_b64_v4i16((LAS v4s*)p));
}
__device__ __forceinline__ bf16x8 mk8(v2u a, v2u b) { v4u t = {a.x, a.y, b.x, b.y}; return __builtin_bit_cast(bf16x8, t); }
__device__ __forceinline__ bf16x8 as8(v4u t) { return __builtin_bit_cast(bf16x8, t); }
__device__ __forceinline__ f32x4 mfma16(bf16x8 a, bf16x8 b, f32x4 c) { return __builtin_amdgcn_mfma_f32_16x16x32_bf16(a, b, c, 0, 0, 0); }

__device__ __forceinline__ float wave_sum(float s) {
    s += dppf<0xB1>(s); s += dppf<0x4E>(s); s += dppf<0x141>(s); s += dppf<0x128>(s);
    return xrow16_sum(s);
}
__device__ __forceinline__ v4u layernorm_vals(v4u raw) {
    float x[8];
#pragma unroll
    for (int i = 0; i < 4; ++i) { x[2 * i] = lo16f(raw[i]); x[2 * i + 1] = hi16f(raw[i]); }
    float s = ((x[0] + x[1]) + (x[2] + x[3])) + ((x[4] + x[5]) + (x[6] + x[7]));
    const float mean = wave_sum(s) * (1.0f / DM);
    float q = 0.f;
#pragma unroll
    for (int i = 0; i < 8; ++i) { float d = x[i] - mean; q += d * d; }
    q = wave_sum(q);
    const float mb = bfr(mean);
    const float vb = bfr(q * (1.0f / DM));
    const float den = bfr(sqrtf(bfr(vb + 1e-5f)));
    v4u outv;
#pragma unroll
    for (int i = 0; i < 4; ++i) {
        float a = bfr(x[2 * i] - mb) / den, b = bfr(x[2 * i + 1] - mb) / den;
        outv[i] = pack2(a, b);
    }
    return outv;
}
__device__ __forceinline__ void layernorm_row(const u16* __restrict__ src, u16* __restrict__ dst, int lane) {
    *(v4u*)(dst + lane * 8) = layernorm_vals(*(const v4u*)(src + lane * 8));
}

__global__ void __launch_bounds__(256) k_prep(const u16* __restrict__ x, u16* __restrict__ xn, const u16* __restrict__ mem0, u16* __restrict__ memx,
                                              v4u* __restrict__ sent, int n_sent16, int memx_off16, v4u* __restrict__ flags, int n_flag16) {
    const int wave = threadIdx.x >> 6, lane = threadIdx.x & 63;
    const int row = blockIdx.x * 4 + wave;
    if (row < S_) layernorm_row(x + (size_t)row * DM, xn + (size_t)row * DM, lane);
    const v4u ones = {0xFFFFFFFFu, 0xFFFFFFFFu, 0xFFFFFFFFu, 0xFFFFFFFFu};
    const int gt = blockIdx.x * 256 + threadIdx.x, nt = gridDim.x * 256;
    for (int i = gt; i < n_sent16; i += nt)
        if (i < memx_off16 || i >= memx_off16 + 512) sent[i] = ones;
    for (int i = gt; i < n_flag16; i += nt) flags[i] = (v4u){0u, 0u, 0u, 0u};
    if (blockIdx.x == 0) {
        for (int i = threadIdx.x; i < MEMN * DM; i += 256) {
            int k = i >> 3, r = i & 7;
            u16 v = mem0[r * DM + k];
            if (v == 0xFFFFu) v = 0x7FFFu;
            memx[i] = v;
        }
    }
}

struct GemmArgs {
    const u16* A; const u16* B0; const u16* B1; const u16* B2; const u16* res;
    u16* C0; u16* C1; u16* C2;
    int lda, ldb, K, pad;
};

__device__ __forceinline__ unsigned offB(unsigned row, unsigned ch) { return 256u * row + 16u * (ch ^ (((row & 3) << 2) | ((row >> 2) & 3))); }
__device__ __forceinline__ unsigned offA(unsigned row, unsigned ch) { return 128u * row + 16u * (ch ^ (row & 7)); }

__device__ __forceinline__ unsigned fB(unsigned row) { return (((row >> 1) & 1) | (((row >> 3) & 1) << 1)) << 1; }
template <int MODE, int BM, int NB, int NST>
__global__ void __launch_bounds__(256) k_gemm2(GemmArgs g) {
    extern __shared__ __attribute__((aligned(16))) unsigned char sm[];
    constexpr int MT = BM / 32;
    constexpr int ABYTES = BM * 128, STAGE = ABYTES + NB * 8192;
    constexpr int LA = BM / 32, LPS = LA + 2 * NB;
    const int tid = threadIdx.x, lane = tid & 63;
    const int wave = __builtin_amdgcn_readfirstlane(tid >> 6);
    const int wm = wave >> 1, wn = wave & 1;
    const int ncol = g.pad & 0xFFFF, nz = g.pad >> 16;
    const int per_xcd = gridDim.x >> 3;
    const int lin = (blockIdx.x & 7) * per_xcd + (blockIdx.x >> 3);
    const int panel = lin / ncol, bz = panel % nz;
    const int bm = (panel / nz) * BM, bn = (lin % ncol) * 64;
    const int kbase = bz * g.K;
    const u16* Asrc[LA];
    const u16* Bsrc[NB][2];
#pragma unroll
    for (int i = 0; i < LA; ++i) { int q = tid + 256 * i; int row = q >> 3, ch = (q & 7) ^ (row & 7); Asrc[i] = g.A + (size_t)(bm + row) * g.lda + kbase + ch * 8; }
#pragma unroll
    for (int n = 0; n < NB; ++n)
#pragma unroll
        for (int i = 0; i < 2; ++i) { int q = tid + 256 * i; int row = q >> 3, ch = (q & 7) ^ fB(row); Bsrc[n][i] = (n == 0 ? g.B0 : g.B1) + (size_t)(kbase + row) * g.ldb + bn + ch * 8; }
    const unsigned sm_base = __builtin_amdgcn_readfirstlane(lds_addr(sm)) + wave * 1024;
    auto stage = [&](int kt) {
        const unsigned base = sm_base + (kt % NST) * STAGE;
#pragma unroll
        for (int i = 0; i < LA; ++i) glds16(Asrc[i] + kt * 64, base + i * 4096);
#pragma unroll
        for (int n = 0; n < NB; ++n)
#pragma unroll
            for (int i = 0; i < 2; ++i) glds16(Bsrc[n][i] + (size_t)kt * 64 * g.ldb, base + ABYTES + n * 8192 + i * 4096);
    };
    f32x4 acc[NB][MT][2];
#pragma unroll
    for (int n = 0; n < NB; ++n)
#pragma unroll
        for (int i = 0; i < MT; ++i) { acc[n][i][0] = (f32x4){0.f, 0.f, 0.f, 0.f}; acc[n][i][1] = acc[n][i][0]; }
    const int nk = g.K / 64;
#pragma unroll
    for (int t = 0; t < NST - 1; ++t) stage(t);
    const int G = lane >> 4, l15 = lane & 15, tq = l15 >> 2, tp = lane & 3;
    for (int kt = 0; kt < nk; ++kt) {
        if (kt + NST - 1 <= nk) asm volatile("s_waitcnt vmcnt(%0)" :: "i"((NST - 2) * LPS) : "memory");
        else asm volatile("s_waitcnt vmcnt(0)" ::: "memory");
        asm volatile("s_waitcnt lgkmcnt(0)" ::: "memory");
        __builtin_amdgcn_s_barrier();
        if (kt + NST - 1 < nk) stage(kt + NST - 1);
        const unsigned char* sA = sm + (kt % NST) * STAGE;
        const unsigned char* sB = sA + ABYTES;
#pragma unroll
        for (int ks = 0; ks < 2; ++ks) {
            bf16x8 af[MT], bq[NB][2];
#pragma unroll
            for (int i = 0; i < MT; ++i) af[i] = as8(*(const v4u*)(sA + offA(wm * (BM / 2) + i * 16 + l15, ks * 4 + G)));
#pragma unroll
            for (int n = 0; n < NB; ++n)
#pragma unroll
                for (int j = 0; j < 2; ++j) {
                    const unsigned r0 = 32 * ks + 8 * G + tq, r1 = r0 + 4, ch = (wn * 2 + j) * 2 + (tp >> 1);
                    v2u lo = tr_read(sB + n * 8192 + r0 * 128 + ((ch ^ fB(r0)) * 16) + 8 * (tp & 1));
                    v2u hi = tr_read(sB + n * 8192 + r1 * 128 + ((ch ^ fB(r1)) * 16) + 8 * (tp & 1));
                    bq[n][j] = mk8(lo, hi);
                }
#pragma unroll
            for (int n = 0; n < NB; ++n)
#pragma unroll
                for (int i = 0; i < MT; ++i)
#pragma unroll
                    for (int j = 0; j < 2; ++j) acc[n][i][j] = mfma16(bq[n][j], af[i], acc[n][i][j]);
        }
    }
#pragma unroll
    for (int i = 0; i < MT; ++i)
#pragma unroll
        for (int j = 0; j < 2; ++j) {
            const int row = bm + wm * (BM / 2) + i * 16 + l15;
            const int col = bn + (wn * 2 + j) * 16 + 4 * G;
            if (MODE == 1) {
                float* P = (float*)g.C0 + ((size_t)bz * S_ + row) * DM + col;
                *(f32x4*)P = acc[0][i][j];
            } else if (MODE == 2) {
                unsigned g0 = pack2(acc[0][i][j][0], acc[0][i][j][1]), g1 = pack2(acc[0][i][j][2], acc[0][i][j][3]);
                unsigned u0 = pack2(acc[NB - 1][i][j][0], acc[NB - 1][i][j][1]), u1 = pack2(acc[NB - 1][i][j][2], acc[NB - 1][i][j][3]);
                v2u v = {pack2(lo16f(g0) * lo16f(u0), hi16f(g0) * hi16f(u0)), pack2(lo16f(g1) * lo16f(u1), hi16f(g1) * hi16f(u1))};
                *(v2u*)(g.C0 + (size_t)row * FF + col) = v;
            } else {
                v2u r = *(const v2u*)(g.res + (size_t)row * DM + col);
                unsigned a0 = pack2(acc[0][i][j][0], acc[0][i][j][1]), a1 = pack2(acc[0][i][j][2], acc[0][i][j][3]);
                v2u v = {pack2(lo16f(r.x) + lo16f(a0), hi16f(r.x) + hi16f(a0)), pack2(lo16f(r.y) + lo16f(a1), hi16f(r.y) + hi16f(a1))};
                *(v2u*)(g.C0 + (size_t)row * DM + col) = v;
            }
        }
}

template <int NZ>
__global__ void __launch_bounds__(256) k_res_ln(const float* __restrict__ P, const u16* __restrict__ x, u16* __restrict__ hb, u16* __restrict__ hn) {
    const int wave = threadIdx.x >> 6, lane = threadIdx.x & 63;
    const int row = blockIdx.x * 4 + wave;
    f32x4 s0 = *(const f32x4*)(P + (size_t)row * DM + lane * 8), s1 = *(const f32x4*)(P + (size_t)row * DM + lane * 8 + 4);
#pragma unroll
    for (int z = 1; z < NZ; ++z) { s0 += *(const f32x4*)(P + ((size_t)z * S_ + row) * DM + lane * 8); s1 += *(const f32x4*)(P + ((size_t)z * S_ + row) * DM + lane * 8 + 4); }
    v4u raw = *(const v4u*)(x + (size_t)row * DM + lane * 8);
    unsigned a0 = pack2(s0[0], s0[1]), a1 = pack2(s0[2], s0[3]), a2 = pack2(s1[0], s1[1]), a3 = pack2(s1[2], s1[3]);
    v4u hv = {pack2(lo16f(raw.x) + lo16f(a0), hi16f(raw.x) + hi16f(a0)), pack2(lo16f(raw.y) + lo16f(a1), hi16f(raw.y) + hi16f(a1)),
              pack2(lo16f(raw.z) + lo16f(a2), hi16f(raw.z) + hi16f(a2)), pack2(lo16f(raw.w) + lo16f(a3), hi16f(raw.w) + hi16f(a3))};
    *(v4u*)(hb + (size_t)row * DM + lane * 8) = hv;
    *(v4u*)(hn + (size_t)row * DM + lane * 8) = layernorm_vals(hv);
}

#define KX_DMA 0
#define XDELAY 0
#define YDELAY 0
constexpr int NX = 64;
constexpr int MEMX_STEP = 8192;
constexpr int PART_BUF = 64 * 64 * 128;
constexpr unsigned SPIN_CAP = 1u << 22;

#define STAMP(i) do {} while (0)
#define DIAG_DECL
#define DIAG_END
struct RecArgs {
    const u16* Wo;
    u16* K; u16* VT; u16* Q; u16* O;
    u16* KM; u16* VMT;
    unsigned char* memx; unsigned char* part;
    const u16* xn; const u16* Wq; const u16* Wk; const u16* Wv;
    unsigned* flags;
};
constexpr int NWORK = 128;
constexpr int FLAG_STRIDE = 16;

__device__ __forceinline__ void wait_flag(const unsigned* f) {
    unsigned spins = 0;
    while (__builtin_amdgcn_readfirstlane(__hip_atomic_load(f, __ATOMIC_RELAXED, __HIP_MEMORY_SCOPE_AGENT)) == 0u) {
        __builtin_amdgcn_s_sleep(2);
        if (++spins > SPIN_CAP) break;
    }
    asm volatile("" ::: "memory");
}

__device__ __forceinline__ void proj_tile(const RecArgs& a, unsigned char* lds, int rb, int c) {
    constexpr int NST = 4;
    const int tid = threadIdx.x, lane = tid & 63;
    const int wave = __builtin_amdgcn_readfirstlane(tid >> 6);
    const int wm = wave >> 1, wn = wave & 1;
    const int mat = c >> 5, head = c & 31;
    const u16* Bp = (mat == 0 ? a.Wk : (mat == 1 ? a.Wv : a.Wq)) + head * DH;
    const u16* Ap = a.xn + (size_t)rb * 128 * DM;
    const bool isv = (mat == 1);
    f32x4 acc[4][4];
#pragma unroll
    for (int i = 0; i < 4; ++i)
#pragma unroll
        for (int j = 0; j < 4; ++j) acc[i][j] = (f32x4){0.f, 0.f, 0.f, 0.f};
    const u16* asrc[4]; const u16* bsrc[4];
#pragma unroll
    for (int i = 0; i < 4; ++i) {
        const int q = tid + 256 * i;
        asrc[i] = Ap + (size_t)(q >> 3) * DM + (((q & 7) ^ ((q >> 3) & 7)) * 8);
        const int r = q >> 4;
        bsrc[i] = Bp + (size_t)r * HD + (((q & 15) ^ (((r & 3) << 2) | ((r >> 2) & 3))) * 8);
    }
    const unsigned sbase = __builtin_amdgcn_readfirstlane(lds_addr(lds)) + wave * 1024;
    auto stage = [&](int kt) {
        const unsigned d = sbase + (kt & (NST - 1)) * 32768;
#pragma unroll
        for (int i = 0; i < 4; ++i) glds16(asrc[i] + kt * 64, d + i * 4096);
#pragma unroll
        for (int i = 0; i < 4; ++i) glds16(bsrc[i] + (size_t)kt * 64 * HD, d + 16384 + i * 4096);
    };
    stage(0); stage(1); stage(2);
    const int G = lane >> 4, l15 = lane & 15, tq = l15 >> 2, tp = lane & 3;
#pragma unroll 1
    for (int kt = 0; kt < 8; ++kt) {
        if (kt <= 5) asm volatile("s_waitcnt vmcnt(16)" ::: "memory");
        else if (kt == 6) asm volatile("s_waitcnt vmcnt(8)" ::: "memory");
        else asm volatile("s_waitcnt vmcnt(0)" ::: "memory");
        asm volatile("s_waitcnt lgkmcnt(0)" ::: "memory");
        __builtin_amdgcn_s_barrier();
        if (kt + 3 < 8) stage(kt + 3);
        const unsigned char* sA = lds + (kt & (NST - 1)) * 32768;
        const unsigned char* sB = sA + 16384;
#pragma unroll
        for (int ks = 0; ks < 2; ++ks) {
            bf16x8 af[4], bfr8[4];
#pragma unroll
            for (int i = 0; i < 4; ++i) af[i] = as8(*(const v4u*)(sA + offA(wm * 64 + i * 16 + l15, ks * 4 + G)));
#pragma unroll
            for (int j = 0; j < 4; ++j) {
                const int cc = wn * 4 + j;
                v2u lo = tr_read(sB + offB(32 * ks + 8 * G + tq, 2 * cc + (tp >> 1)) + 8 * (tp & 1));
                v2u hi = tr_read(sB + offB(32 * ks + 8 * G + 4 + tq, 2 * cc + (tp >> 1)) + 8 * (tp & 1));
                bfr8[j] = mk8(lo, hi);
            }
            if (isv) {
#pragma unroll
                for (int i = 0; i < 4; ++i)
#pragma unroll
                    for (int j = 0; j < 4; ++j) acc[i][j] = mfma16(af[i], bfr8[j], acc[i][j]);
            } else {
#pragma unroll
                for (int i = 0; i < 4; ++i)
#pragma unroll
                    for (int j = 0; j < 4; ++j) acc[i][j] = mfma16(bfr8[j], af[i], acc[i][j]);
            }
        }
    }
    __syncthreads();
    unsigned char* img = lds;
#pragma unroll
    for (int i = 0; i < 4; ++i)
#pragma unroll
        for (int j = 0; j < 4; ++j) {
            v2u v = {pack2(acc[i][j][0], acc[i][j][1]), pack2(acc[i][j][2], acc[i][j][3])};
            if (isv) {
                const int e = (wn * 4 + j) * 16 + l15, row0 = wm * 64 + i * 16 + 4 * G;
                const int line = (row0 >> 5) * 128 + e;
                *(v2u*)(img + line * 64 + ((((row0 & 31) >> 2) ^ (e & 7)) * 8)) = v;
            } else {
                const int row = wm * 64 + i * 16 + l15, col0 = (wn * 4 + j) * 16 + 4 * G;
                *(v2u*)(img + row * 256 + (((col0 >> 3) ^ (row & 15)) * 16) + ((col0 & 4) * 2)) = v;
            }
        }
    __syncthreads();
    if (isv) {
        const __amdgpu_buffer_rsrc_t r = __builtin_amdgcn_make_buffer_rsrc(a.VT, 0, S_ * HD * 2, 0x00020000);
#pragma unroll
        for (int i = 0; i < 8; ++i) {
            const int q = tid + 256 * i;
            const int line = q >> 2, cq = q & 3, e = line & 127;
            v2u x0 = *(const v2u*)(img + line * 64 + (((2 * cq) ^ (e & 7)) * 8));
            v2u x1 = *(const v2u*)(img + line * 64 + (((2 * cq + 1) ^ (e & 7)) * 8));
            v4u v = {x0.x, x0.y, x1.x, x1.y};
            const int seg = rb * 4 + (line >> 7);
            __builtin_amdgcn_raw_buffer_store_b128(v, r, (unsigned)((((seg * NH + head) * DH + e) * 32 + cq * 8) * 2), 0, 16);
        }
    } else {
        const __amdgpu_buffer_rsrc_t r = __builtin_amdgcn_make_buffer_rsrc(mat == 0 ? a.K : a.Q, 0, S_ * HD * 2, 0x00020000);
#pragma unroll
        for (int i = 0; i < 8; ++i) {
            const int q = tid + 256 * i;
            const int row = q >> 4, ch = q & 15;
            v4u v = *(const v4u*)(img + row * 256 + ((ch ^ (row & 15)) * 16));
            __builtin_amdgcn_raw_buffer_store_b128(v, r, (unsigned)(((rb * 128 + row) * HD + head * DH + ch * 8) * 2), 0, 16);
        }
    }
    asm volatile("s_waitcnt vmcnt(0)" ::: "memory");
    __syncthreads();
    if (tid == 0) __hip_atomic_store(a.flags + (rb * 96 + c) * FLAG_STRIDE, 1u, __ATOMIC_RELAXED, __HIP_MEMORY_SCOPE_AGENT);
}

__device__ __forceinline__ void attn_unit(const RecArgs& a, int seg, int h, int lane) {
    const int G = lane >> 4, l15 = lane & 15;
    const float scale = 0.08838834764831845f;
    const __amdgpu_buffer_rsrc_t rq = __builtin_amdgcn_make_buffer_rsrc(a.Q, 0, S_ * HD * 2, 0x00020000);
    const __amdgpu_buffer_rsrc_t rk = __builtin_amdgcn_make_buffer_rsrc(a.K, 0, S_ * HD * 2, 0x00020000);
    const __amdgpu_buffer_rsrc_t rv = __builtin_amdgcn_make_buffer_rsrc(a.VT, 0, S_ * HD * 2, 0x00020000);
    const __amdgpu_buffer_rsrc_t rkm = __builtin_amdgcn_make_buffer_rsrc(a.KM, 0, NSEG * 8 * HD * 2, 0x00020000);
    const __amdgpu_buffer_rsrc_t rvm = __builtin_amdgcn_make_buffer_rsrc(a.VMT, 0, NSEG * 8 * HD * 2, 0x00020000);
    const int rb = seg >> 2;
    wait_flag(a.flags + (rb * 96 + h) * FLAG_STRIDE);
    wait_flag(a.flags + (rb * 96 + 32 + h) * FLAG_STRIDE);
    wait_flag(a.flags + (rb * 96 + 64 + h) * FLAG_STRIDE);
    v4u k1[4], k2[4], q0[4], q1[4];
    v2u v1[8], v2[8];
#pragma unroll
    for (int ks = 0; ks < 4; ++ks) {
        const unsigned eo = (unsigned)(h * DH + ks * 32 + 8 * G) * 2;
        k1[ks] = __builtin_amdgcn_raw_buffer_load_b128(rk, (unsigned)(seg * 32 + l15) * (HD * 2) + eo, 0, 16);
        k2[ks] = __builtin_amdgcn_raw_buffer_load_b128(rk, (unsigned)(seg * 32 + 16 + l15) * (HD * 2) + eo, 0, 16);
        q0[ks] = __builtin_amdgcn_raw_buffer_load_b128(rq, (unsigned)(seg * 32 + l15) * (HD * 2) + eo, 0, 16);
        q1[ks] = __builtin_amdgcn_raw_buffer_load_b128(rq, (unsigned)(seg * 32 + 16 + l15) * (HD * 2) + eo, 0, 16);
    }
#pragma unroll
    for (int et = 0; et < 8; ++et) {
        const unsigned vo = (unsigned)(((seg * NH + h) * DH + et * 16 + l15) * 32 + 4 * G) * 2;
        v1[et] = __builtin_amdgcn_raw_buffer_load_b64(rv, vo, 0, 16);
        v2[et] = __builtin_amdgcn_raw_buffer_load_b64(rv, vo + 32, 0, 16);
    }
    v4u k0[4]; v2u vm[8];
    {
        unsigned spins = 0;
        const unsigned* probe = (const unsigned*)(a.VMT + ((size_t)(seg * NH + h) * DH + 127) * 8);
        while (__builtin_amdgcn_readfirstlane(__hip_atomic_load(probe, __ATOMIC_RELAXED, __HIP_MEMORY_SCOPE_AGENT)) == 0xFFFFFFFFu) {
            __builtin_amdgcn_s_sleep(100);
            if (++spins > SPIN_CAP) break;
        }
        for (;;) {
            unsigned mx = 0;
#pragma unroll
            for (int ks = 0; ks < 4; ++ks) {
                k0[ks] = __builtin_amdgcn_raw_buffer_load_b128(rkm, (unsigned)((seg * 8 + (l15 & 7)) * HD + h * DH + ks * 32 + 8 * G) * 2, 0, 16);
            }
#pragma unroll
            for (int et = 0; et < 8; ++et) vm[et] = __builtin_amdgcn_raw_buffer_load_b64(rvm, (unsigned)(((seg * NH + h) * DH + et * 16 + l15) * 8 + 4 * (G & 1)) * 2, 0, 16);
#pragma unroll
            for (int ks = 0; ks < 4; ++ks) mx = max(mx, umax4(k0[ks]));
#pragma unroll
            for (int et = 0; et < 8; ++et) mx = umax3(mx, vm[et].x, vm[et].y);
            asm volatile("" ::: "memory");
            if (__all(mx != 0xFFFFFFFFu)) break;
            __builtin_amdgcn_s_sleep(16);
            if (++spins > SPIN_CAP) break;
        }
    }
    f32x4 st[3][2];
#pragma unroll
    for (int i = 0; i < 3; ++i) { st[i][0] = (f32x4){0.f, 0.f, 0.f, 0.f}; st[i][1] = st[i][0]; }
#pragma unroll
    for (int ks = 0; ks < 4; ++ks) {
        st[0][0] = mfma16(as8(k0[ks]), as8(q0[ks]), st[0][0]); st[0][1] = mfma16(as8(k0[ks]), as8(q1[ks]), st[0][1]);
        st[1][0] = mfma16(as8(k1[ks]), as8(q0[ks]), st[1][0]); st[1][1] = mfma16(as8(k1[ks]), as8(q1[ks]), st[1][1]);
        st[2][0] = mfma16(as8(k2[ks]), as8(q0[ks]), st[2][0]); st[2][1] = mfma16(as8(k2[ks]), as8(q1[ks]), st[2][1]);
    }
    bf16x8 pf[2][2];
#pragma unroll
    for (int qt = 0; qt < 2; ++qt) {
        const int t = qt * 16 + l15;
        float v[3][4];
        float m = -3.0e38f;
#pragma unroll
        for (int i = 0; i < 3; ++i)
#pragma unroll
            for (int r = 0; r < 4; ++r) {
                float sc = bfr(bfr(st[i][qt][r]) * scale);
                bool ok;
                if (i == 0) ok = (G < 2); else ok = ((i - 1) * 16 + 4 * G + r) <= t;
                bool exist = (i != 0) || (G < 2);
                sc = ok ? sc : bfr(-1.0e9f);
                v[i][r] = exist ? sc : -3.0e38f;
                m = fmaxf(m, v[i][r]);
            }
        m = xrow16_max(m);
        float sum = 0.f;
#pragma unroll
        for (int i = 0; i < 3; ++i)
#pragma unroll
            for (int r = 0; r < 4; ++r) { bool exist = (i != 0) || (G < 2); v[i][r] = exist ? __expf(v[i][r] - m) : 0.f; sum += v[i][r]; }
        sum = xrow16_sum(sum);
        const float inv = 1.0f / sum;
        v4u p0 = {pack2(v[0][0] * inv, v[0][1] * inv), pack2(v[0][2] * inv, v[0][3] * inv), pack2(v[1][0] * inv, v[1][1] * inv), pack2(v[1][2] * inv, v[1][3] * inv)};
        v4u p1 = {pack2(v[2][0] * inv, v[2][1] * inv), pack2(v[2][2] * inv, v[2][3] * inv), 0u, 0u};
        pf[qt][0] = as8(p0); pf[qt][1] = as8(p1);
    }
#pragma unroll
    for (int et = 0; et < 8; ++et) {
        bf16x8 a0 = mk8(vm[et], v1[et]);
        bf16x8 a1 = mk8(v2[et], (v2u){0u, 0u});
#pragma unroll
        for (int qt = 0; qt < 2; ++qt) {
            f32x4 o = (f32x4){0.f, 0.f, 0.f, 0.f};
            o = mfma16(a0, pf[qt][0], o);
            o = mfma16(a1, pf[qt][1], o);
            v2u ov = {pack2(o[0], o[1]), pack2(o[2], o[3])};
            *(v2u*)(a.O + ((size_t)seg * 32 + qt * 16 + l15) * HD + h * DH + et * 16 + 4 * G) = ov;
        }
    }
}

constexpr int L_WO = 0;
constexpr int L_MEM = 65536;
constexpr int L_T = L_MEM + 8192;
constexpr int L_VM = L_T + 4096;
constexpr int L_KX = L_VM + 2048;
constexpr int L_VX = L_KX + 8192;
constexpr int L_STG = 96 * 1024;
constexpr int L_TOTAL = 160 * 1024;

__global__ void __launch_bounds__(256, 1) k_rec(RecArgs a) {
    extern __shared__ __attribute__((aligned(16))) unsigned char lds[];
    const int tid = threadIdx.x, lane = tid & 63;
    const int wave = __builtin_amdgcn_readfirstlane(tid >> 6);
    const int G = lane >> 4, l15 = lane & 15;
    const __amdgpu_buffer_rsrc_t rmem = __builtin_amdgcn_make_buffer_rsrc(a.memx, 0, (NSEG + 1) * MEMX_STEP, 0x00020000);
    const __amdgpu_buffer_rsrc_t rpart = __builtin_amdgcn_make_buffer_rsrc(a.part, 0, 2 * PART_BUF, 0x00020000);

    if (blockIdx.x >= 2 * NX) {
        const int w = blockIdx.x - 2 * NX;
        for (int i = w; i < 16 * 96; i += NWORK) proj_tile(a, lds, i / 96, i % 96);
        for (int u = w; u < NSEG * 8; u += NWORK) attn_unit(a, u >> 3, (u & 7) * 4 + wave, lane);
        return;
    }
    if (blockIdx.x >= NX) {
        if (wave != 0) return;
        const int y = blockIdx.x - NX;
        DIAG_DECL
        unsigned long long typub = 0;
        bf16x8 sel;
#pragma unroll
        for (int j = 0; j < 8; ++j) sel[j] = (l15 == j) ? (__bf16)1.0f : (__bf16)0.0f;
        for (int s = 0; s < NSEG; ++s) {
            const unsigned base = (s & 1) * PART_BUF + y * 8192 + lane * 16;
            v4u m[8];
            unsigned spins = 0;
            for (;;) {
#pragma unroll
                for (int t = 0; t < 8; ++t) m[t] = __builtin_amdgcn_raw_buffer_load_b128(rpart, base + t * 1024, 0, 16);
                unsigned mx = 0;
#pragma unroll
                for (int t = 0; t < 8; ++t) mx = umax3(mx, umax3(m[t].x, m[t].y, m[t].z), m[t].w);
                asm volatile("" ::: "memory");
                if (__all(mx != 0xFFFFFFFFu)) break;
                if (++spins > SPIN_CAP) break;
            }
            STAMP(0);
            f32x4 acc0 = (f32x4){0.f, 0.f, 0.f, 0.f}, acc1 = acc0;
#pragma unroll
            for (int t = 0; t < 8; t += 2) { acc0 = mfma16(sel, as8(m[t]), acc0); acc1 = mfma16(sel, as8(m[t + 1]), acc1); }
            acc0 += acc1;
#pragma unroll
            for (int r = 0; r < 4; ++r) acc0[r] += dppf<0x128>(acc0[r]);
            if (lane < 32 && l15 < 8) {
                v2u v = {nosent(pack2(acc0[0], acc0[1])), nosent(pack2(acc0[2], acc0[3]))};
                __builtin_amdgcn_raw_buffer_store_b64(v, rmem, (s + 1) * MEMX_STEP + y * 128 + l15 * 16 + G * 8, 0, 16);
            }
            STAMP(1);
            const v4u sent = {0xFFFFFFFFu, 0xFFFFFFFFu, 0xFFFFFFFFu, 0xFFFFFFFFu};
#pragma unroll
            for (int t = 0; t < 8; ++t) __builtin_amdgcn_raw_buffer_store_b128(sent, rpart, base + t * 1024, 0, 16);
            asm volatile("s_waitcnt vmcnt(0)" ::: "memory");
        }
        DIAG_END;
        return;
    }

    const int b = blockIdx.x, h = b >> 1, g = b & 1;
    bf16x16 wqk[2][16];
    bf16x8 wv[16];
    {
        const int tq_ = l15 >> 2, tp_ = lane & 3;
        const unsigned stg = __builtin_amdgcn_readfirstlane(lds_addr(lds)) + wave * 1024;
        int srow[4], sch[4];
#pragma unroll
        for (int i = 0; i < 4; ++i) { const int q = tid + 256 * i; srow[i] = q >> 4; sch[i] = ((q & 15) ^ (((srow[i] & 3) << 2) | ((srow[i] >> 2) & 3))) * 8; }
        auto issue = [&](int c) {
            const u16* s0; const u16* s1; int ld;
            if (c < 8) { s0 = a.Wq + (size_t)(64 * c) * HD + h * DH; s1 = a.Wk + (size_t)(64 * c) * HD + h * DH; ld = HD; }
            else if (c < 12) { s0 = a.Wv + (size_t)(128 * (c - 8)) * HD + h * DH; s1 = s0 + (size_t)64 * HD; ld = HD; }
            else { s0 = a.Wo + (size_t)(h * DH + 64 * g) * DM + 256 * (c - 12); s1 = s0 + 128; ld = DM; }
            const unsigned dst = stg + (((c & 3) < 2) ? (unsigned)(L_STG + (c & 1) * 32768) : (unsigned)(L_WO + (c & 1) * 32768));
#pragma unroll
            for (int i = 0; i < 4; ++i) glds16(s0 + (size_t)srow[i] * ld + sch[i], dst + i * 4096);
#pragma unroll
            for (int i = 0; i < 4; ++i) glds16(s1 + (size_t)srow[i] * ld + sch[i], dst + 16384 + i * 4096);
        };
        issue(0); issue(1); issue(2);
#pragma unroll
        for (int c = 0; c < 14; ++c) {
            if (c + 3 < 14) { issue(c + 3); asm volatile("s_waitcnt vmcnt(24)" ::: "memory"); }
            else if (c + 3 == 14) asm volatile("s_waitcnt vmcnt(16)" ::: "memory");
            else if (c + 2 == 14) asm volatile("s_waitcnt vmcnt(8)" ::: "memory");
            else asm volatile("s_waitcnt vmcnt(0)" ::: "memory");
            __builtin_amdgcn_s_barrier();
            const unsigned char* t0 = lds + (((c & 3) < 2) ? (L_STG + (c & 1) * 32768) : (L_WO + (c & 1) * 32768));
            const unsigned char* t1 = t0 + 16384;
            if (c < 8) {
#pragma unroll
                for (int tt = 0; tt < 2; ++tt)
#pragma unroll
                    for (int u = 0; u < 2; ++u) {
                        v4u hf[2];
#pragma unroll
                        for (int half = 0; half < 2; ++half) {
                            const unsigned off = offB(32 * tt + 16 * half + 4 * G + tq_, 2 * (2 * wave + u) + (tp_ >> 1)) + 8 * (tp_ & 1);
                            v2u q = tr_read(t0 + off), k = tr_read(t1 + off);
                            hf[half] = (v4u){q.x, k.x, q.y, k.y};
                        }
                        bf16x8 lo = as8(hf[0]), hi = as8(hf[1]);
#pragma unroll
                        for (int j = 0; j < 8; ++j) { wqk[u][2 * c + tt][j] = lo[j]; wqk[u][2 * c + tt][8 + j] = hi[j]; }
                    }
            } else if (c < 12) {
#pragma unroll
                for (int j = 0; j < 2; ++j)
#pragma unroll
                    for (int tt = 0; tt < 2; ++tt) {
                        const unsigned char* tb = j ? t1 : t0;
                        v2u lo = tr_read(tb + offB(32 * tt + 8 * G + tq_, 2 * (4 * g + wave) + (tp_ >> 1)) + 8 * (tp_ & 1));
                        v2u hi = tr_read(tb + offB(32 * tt + 8 * G + 4 + tq_, 2 * (4 * g + wave) + (tp_ >> 1)) + 8 * (tp_ & 1));
                        wv[4 * (c - 8) + 2 * j + tt] = mk8(lo, hi);
                    }
            } else {
#pragma unroll
                for (int j = 0; j < 2; ++j)
#pragma unroll
                    for (int kk = 0; kk < 2; ++kk)
#pragma unroll
                        for (int half = 0; half < 2; ++half) {
                            const unsigned char* tb = j ? t1 : t0;
                            const int e0 = 32 * kk + 16 * (G & 1) + 4 * (2 * half + (G >> 1)) + tq_;
                            v2u A = tr_read(tb + offB(e0, 4 * wave + (tp_ >> 1)) + 8 * (tp_ & 1));
                            v2u B = tr_read(tb + offB(e0, 4 * wave + 2 + (tp_ >> 1)) + 8 * (tp_ & 1));
                            const int dp = 8 * (c - 12) + 4 * j + wave;
                            *(v4u*)(lds + L_WO + ((dp * 2 + kk) * 2 + half) * 1024 + lane * 16) = (v4u){A.x, B.x, A.y, B.y};
                        }
            }
            asm volatile("s_waitcnt lgkmcnt(0)" ::: "memory");
            __builtin_amdgcn_s_barrier();
        }
    }
    const int sidx = (lane & 8) ? 0xEEEE : 0x4444;
    const int tq = l15 >> 2, tp = lane & 3;
    const unsigned char* memrd = lds + L_MEM + (8 * G + tq) * 16 + (tp & 1) * 8;
    const unsigned char* trd = lds + L_T + (8 * G + tq) * 32 + (tp & 1) * 8;
    const unsigned char* wp = lds + L_WO + wave * 16384 + lane * 16;
    const float sl2e = 0.08838834764831845f * 1.4426950408889634f;
    const bool exist0 = (G < 2);
    constexpr int PD = 4;
    const __amdgpu_buffer_rsrc_t rk = __builtin_amdgcn_make_buffer_rsrc(a.K, 0, S_ * HD * 2, 0x00020000);
    const __amdgpu_buffer_rsrc_t rv = __builtin_amdgcn_make_buffer_rsrc(a.VT, 0, S_ * HD * 2, 0x00020000);
    const __amdgpu_buffer_rsrc_t rkm = __builtin_amdgcn_make_buffer_rsrc(a.KM, 0, NSEG * 8 * HD * 2, 0x00020000);
    const __amdgpu_buffer_rsrc_t rvm = __builtin_amdgcn_make_buffer_rsrc(a.VMT, 0, NSEG * 8 * HD * 2, 0x00020000);

    wait_flag(a.flags + h * FLAG_STRIDE);
    wait_flag(a.flags + (32 + h) * FLAG_STRIDE);
    const unsigned lds0 = __builtin_amdgcn_readfirstlane(lds_addr(lds));
    const u16* ksrc0; const u16* ksrc1; const u16* vsrc;
    {
        const int p0 = tid, p1 = tid + 256;
        ksrc0 = a.K + (size_t)(p0 >> 4) * HD + h * DH + (((p0 & 15) ^ ((p0 >> 4) & 15)) * 8);
        ksrc1 = a.K + (size_t)(p1 >> 4) * HD + h * DH + (((p1 & 15) ^ ((p1 >> 4) & 15)) * 8);
        vsrc = a.VT + ((size_t)h * DH + 64 * g) * 32 + tid * 8;
    }
    auto load_kv = [&](int s) {
        glds16_sc1(ksrc0 + (size_t)s * 32 * HD, lds0 + L_KX + wave * 1024);
        glds16_sc1(ksrc1 + (size_t)s * 32 * HD, lds0 + L_KX + 4096 + wave * 1024);
        glds16_sc1(vsrc + (size_t)s * NH * DH * 32, lds0 + L_VX + wave * 1024);
    };
    v4u kx[2][4]; v2u vx[2];
    auto load_kx = [&](int s) {
#pragma unroll
        for (int kt = 0; kt < 2; ++kt)
#pragma unroll
            for (int ks = 0; ks < 4; ++ks) kx[kt][ks] = __builtin_amdgcn_raw_buffer_load_b128(rk, (unsigned)((s * 32 + kt * 16 + l15) * HD + h * DH + ks * 32 + 8 * G) * 2, 0, 16);
        const unsigned vo = (unsigned)(((s * NH + h) * DH + 64 * g + wave * 16 + l15) * 32 + 4 * G) * 2;
        vx[0] = __builtin_amdgcn_raw_buffer_load_b64(rv, vo, 0, 16); vx[1] = __builtin_amdgcn_raw_buffer_load_b64(rv, vo + 32, 0, 16);
    };
    load_kx(0);
    unsigned long long tpub = 0;
    DIAG_DECL
    for (int s = 0; s < NSEG; ++s) {
        const int sn = (s + 1 < NSEG) ? s + 1 : s;
        {
            v4u m0, m1;
            unsigned spins = 0;
            for (;;) {
                m0 = __builtin_amdgcn_raw_buffer_load_b128(rmem, s * MEMX_STEP + tid * 16, 0, 16);
                m1 = __builtin_amdgcn_raw_buffer_load_b128(rmem, s * MEMX_STEP + 4096 + tid * 16, 0, 16);
                unsigned mx = umax3(umax3(m0.x, m0.y, m0.z), umax3(m0.w, m1.x, m1.y), max(m1.z, m1.w));
                asm volatile("" ::: "memory");
                if (__all(mx != 0xFFFFFFFFu)) break;
                if (++spins > SPIN_CAP) break;
            }
            *(v4u*)(lds + L_MEM + tid * 16) = m0;
            *(v4u*)(lds + L_MEM + 4096 + tid * 16) = m1;
            asm volatile("s_waitcnt vmcnt(0)" ::: "memory");
        }
        STAMP(0);
        __syncthreads();
        STAMP(1);
        v2u lo[16], hi[16];
        f32x4 acc0 = (f32x4){0.f, 0.f, 0.f, 0.f}, acc1 = acc0;
#pragma unroll
        for (int t = 0; t < PD; ++t) { lo[t] = tr_read(memrd + t * 512); hi[t] = tr_read(memrd + t * 512 + 64); }
        SB0();
#pragma unroll
        for (int t = 0; t < 16; ++t) {
            if (t + PD < 16) { lo[t + PD] = tr_read(memrd + (t + PD) * 512); hi[t + PD] = tr_read(memrd + (t + PD) * 512 + 64); }
            acc0 = __builtin_amdgcn_smfmac_f32_16x16x64_bf16(mk8(lo[t], hi[t]), wqk[0][t], acc0, sidx, 0, 0);
            acc1 = __builtin_amdgcn_smfmac_f32_16x16x64_bf16(mk8(lo[t], hi[t]), wqk[1][t], acc1, sidx, 0, 0);
            SB0();
        }
        {
            v2u v0 = {pack2(acc0[0], acc0[1]), pack2(acc0[2], acc0[3])};
            v2u v1 = {pack2(acc1[0], acc1[1]), pack2(acc1[2], acc1[3])};
            *(v2u*)(lds + L_T + (wave * 32 + l15) * 32 + G * 8) = v0;
            *(v2u*)(lds + L_T + (wave * 32 + 16 + l15) * 32 + G * 8) = v1;
        }
        STAMP(2);
        __syncthreads();
        STAMP(3);
        v2u q_lo[4], q_hi[4], k_lo[4], k_hi[4];
#pragma unroll
        for (int ks = 0; ks < 4; ++ks) {
            q_lo[ks] = tr_read(trd + ks * 1024); q_hi[ks] = tr_read(trd + ks * 1024 + 128);
            k_lo[ks] = tr_read(trd + ks * 1024 + 16); k_hi[ks] = tr_read(trd + ks * 1024 + 128 + 16);
        }
        f32x4 accv = (f32x4){0.f, 0.f, 0.f, 0.f};
#pragma unroll
        for (int t = 0; t < 4; ++t) accv = mfma16(mk8(lo[t], hi[t]), wv[t], accv);
        SB0();
        f32x4 st[3];
#pragma unroll
        for (int i = 0; i < 3; ++i) st[i] = (f32x4){0.f, 0.f, 0.f, 0.f};
#pragma unroll
        for (int ks = 0; ks < 4; ++ks) {
            bf16x8 qf = mk8(q_lo[ks], q_hi[ks]);
            st[1] = mfma16(as8(kx[0][ks]), qf, st[1]);
            st[2] = mfma16(as8(kx[1][ks]), qf, st[2]);
            st[0] = mfma16(mk8(k_lo[ks], k_hi[ks]), qf, st[0]);
        }
        SB0();
#pragma unroll
        for (int t = 4; t < 16; ++t) accv = mfma16(mk8(lo[t], hi[t]), wv[t], accv);
        v4u wo[4][2][2];
#pragma unroll
        for (int i = 0; i < 4; ++i) { wo[i][0][0] = *(const v4u*)(wp + i * 4096); wo[i][0][1] = *(const v4u*)(wp + i * 4096 + 1024); }
        float m = fmaxf(fmaxf(fmaxf(st[1][0], st[1][1]), fmaxf(st[1][2], st[1][3])), fmaxf(fmaxf(st[2][0], st[2][1]), fmaxf(st[2][2], st[2][3])));
        float m0 = fmaxf(fmaxf(st[0][0], st[0][1]), fmaxf(st[0][2], st[0][3]));
        m = exist0 ? fmaxf(m, m0) : m;
        m = xrow16_max(m);
        const float nm = -m * sl2e;
        float v[3][4];
#pragma unroll
        for (int i = 0; i < 3; ++i)
#pragma unroll
            for (int r = 0; r < 4; ++r) v[i][r] = __builtin_amdgcn_exp2f(__builtin_fmaf(st[i][r], sl2e, nm));
#pragma unroll
        for (int r = 0; r < 4; ++r) v[0][r] = exist0 ? v[0][r] : 0.f;
        float sum = ((v[0][0] + v[0][1]) + (v[0][2] + v[0][3])) + ((v[1][0] + v[1][1]) + (v[1][2] + v[1][3])) + ((v[2][0] + v[2][1]) + (v[2][2] + v[2][3]));
        sum = xrow16_sum(sum);
        const float inv = __builtin_amdgcn_rcpf(sum);
        v4u p0 = {pack2(v[0][0] * inv, v[0][1] * inv), pack2(v[0][2] * inv, v[0][3] * inv), pack2(v[1][0] * inv, v[1][1] * inv), pack2(v[1][2] * inv, v[1][3] * inv)};
        v4u p1 = {pack2(v[2][0] * inv, v[2][1] * inv), pack2(v[2][2] * inv, v[2][3] * inv), 0u, 0u};
        v2u vm = {pack2(accv[0], accv[1]), pack2(accv[2], accv[3])};
        f32x4 o = mfma16(mk8(vx[1], (v2u){0u, 0u}), as8(p1), (f32x4){0.f, 0.f, 0.f, 0.f});
        o = mfma16(mk8(vm, vx[0]), as8(p0), o);
        *(v2u*)(lds + L_VM + wave * 512 + lane * 8) = (v2u){pack2(o[0], o[1]), pack2(o[2], o[3])};
        STAMP(4);
        __syncthreads();
        STAMP(5);
        v2u ob[4];
#pragma unroll
        for (int et = 0; et < 4; ++et) ob[et] = *(const v2u*)(lds + L_VM + et * 512 + lane * 8);
#pragma unroll
        for (int i = 0; i < 4; ++i) { wo[i][1][0] = *(const v4u*)(wp + i * 4096 + 2048); wo[i][1][1] = *(const v4u*)(wp + i * 4096 + 3072); }
        SB0();
        const bf16x8 oa0 = mk8(ob[0], ob[1]), oa1 = mk8(ob[2], ob[3]);
        f32x4 d[4];
#pragma unroll
        for (int kk = 0; kk < 2; ++kk)
#pragma unroll
            for (int i = 0; i < 4; ++i) {
                bf16x16 wf;
                bf16x8 w0b = as8(wo[i][kk][0]), w1b = as8(wo[i][kk][1]);
#pragma unroll
                for (int j = 0; j < 8; ++j) { wf[j] = w0b[j]; wf[8 + j] = w1b[j]; }
                d[i] = __builtin_amdgcn_smfmac_f32_16x16x64_bf16(kk == 0 ? oa0 : oa1, wf, kk == 0 ? (f32x4){0.f, 0.f, 0.f, 0.f} : d[i], sidx, 0, 0);
            }
#pragma unroll
        for (int i = 0; i < 4; ++i) {
            const int dcol = 32 * (wave * 4 + i) + 16 * (G >> 1) + l15;
            v2u pv = {nosent(pack2(d[i][0], d[i][1])), nosent(pack2(d[i][2], d[i][3]))};
            __builtin_amdgcn_raw_buffer_store_b64(pv, rpart, (s & 1) * PART_BUF + (dcol >> 3) * 8192 + b * 128 + (dcol & 7) * 16 + (G & 1) * 8, 0, 16);
        }
        STAMP(6);
        if (G < 2) {
            v2u vs = {nosent(vm.x), nosent(vm.y)};
            __builtin_amdgcn_raw_buffer_store_b64(vs, rvm, (unsigned)(((s * NH + h) * DH + 64 * g + wave * 16 + l15) * 8 + 4 * G) * 2, 0, 16);
        }
        if (g == 0 && l15 < 8) {
            v2u kl = wave == 0 ? k_lo[0] : (wave == 1 ? k_lo[1] : (wave == 2 ? k_lo[2] : k_lo[3]));
            v2u kh = wave == 0 ? k_hi[0] : (wave == 1 ? k_hi[1] : (wave == 2 ? k_hi[2] : k_hi[3]));
            v4u kv = {nosent(kl.x), nosent(kl.y), nosent(kh.x), nosent(kh.y)};
            __builtin_amdgcn_raw_buffer_store_b128(kv, rkm, (unsigned)((s * 8 + l15) * HD + h * DH + wave * 32 + 8 * G) * 2, 0, 16);
        }
        SB0();
        load_kx(sn);
        if ((s & 3) == 2 && s + 2 < NSEG) {
            wait_flag(a.flags + (((s + 2) >> 2) * 96 + h) * FLAG_STRIDE);
            wait_flag(a.flags + (((s + 2) >> 2) * 96 + 32 + h) * FLAG_STRIDE);
        }
    }
    DIAG_END;
}

extern "C" void kernel_launch(void* const* d_in, const int* in_sizes, int n_in, void* d_out, int out_size, void* d_ws, size_t ws_size,
                              hipStream_t stream) {
    const u16* x = (const u16*)d_in[0];
    const u16* mem0 = (const u16*)d_in[1];
    const u16* Wq = (const u16*)d_in[2];
    const u16* Wk = (const u16*)d_in[3];
    const u16* Wv = (const u16*)d_in[4];
    const u16* Wo = (const u16*)d_in[5];
    const u16* Wg = (const u16*)d_in[8];
    const u16* Wu = (const u16*)d_in[9];
    const u16* Wd = (const u16*)d_in[10];
    u16* out = (u16*)d_out;

    unsigned char* ws = (unsigned char*)d_ws;
    size_t off = 0;
    auto alloc = [&](size_t bytes) { unsigned char* p = ws + off; off += (bytes + 255) & ~(size_t)255; return p; };
    constexpr size_t KM_BYTES = (size_t)NSEG * 8 * HD * 2;
    constexpr size_t SENT_BYTES = (size_t)2 * PART_BUF + (size_t)(NSEG + 1) * MEMX_STEP + 2 * KM_BYTES;
    unsigned* flags = (unsigned*)alloc(16 * 96 * FLAG_STRIDE * 4);
    unsigned char* xchg = alloc(SENT_BYTES);
    unsigned char* part = xchg;
    unsigned char* memx = xchg + 2 * PART_BUF;
    u16* KM = (u16*)(memx + (size_t)(NSEG + 1) * MEMX_STEP);
    u16* VMT = (u16*)((unsigned char*)KM + KM_BYTES);
    u16* xn = (u16*)alloc((size_t)S_ * DM * 2);
    u16* Q = (u16*)alloc((size_t)S_ * HD * 2);
    u16* K = (u16*)alloc((size_t)S_ * HD * 2);
    u16* VT = (u16*)alloc((size_t)S_ * HD * 2);
    u16* O = (u16*)alloc((size_t)S_ * HD * 2);
    u16* hb = (u16*)alloc((size_t)S_ * DM * 2);
    u16* hn = (u16*)alloc((size_t)S_ * DM * 2);
    u16* act = (u16*)alloc((size_t)S_ * FF * 2);
    float* Pk = (float*)alloc((size_t)2 * S_ * DM * 4);

    k_prep<<<S_ / 4, 256, 0, stream>>>(x, xn, mem0, (u16*)memx, (v4u*)xchg, (int)(SENT_BYTES / 16), (int)(2 * PART_BUF / 16), (v4u*)flags, 16 * 96 * FLAG_STRIDE / 4);
    {
        static bool attr_set = false;
        if (!attr_set) { (void)hipFuncSetAttribute((const void*)k_rec, hipFuncAttributeMaxDynamicSharedMemorySize, L_TOTAL); attr_set = true; }
        RecArgs r{Wo, K, VT, Q, O, KM, VMT, memx, part, xn, Wq, Wk, Wv, flags};
        k_rec<<<2 * NX + NWORK, 256, L_TOTAL, stream>>>(r);
    }
    {
        static bool attr2 = false;
        if (!attr2) {
            (void)hipFuncSetAttribute((const void*)k_gemm2<1, 128, 1, 6>, hipFuncAttributeMaxDynamicSharedMemorySize, 6 * (128 * 128 + 8192));
            (void)hipFuncSetAttribute((const void*)k_gemm2<2, 128, 2, 2>, hipFuncAttributeMaxDynamicSharedMemorySize, 2 * (128 * 128 + 16384));
            (void)hipFuncSetAttribute((const void*)k_gemm2<3, 64, 1, 8>, hipFuncAttributeMaxDynamicSharedMemorySize, 8 * (64 * 128 + 8192));
            attr2 = true;
        }
        GemmArgs g1{O, Wo, nullptr, nullptr, nullptr, (u16*)Pk, nullptr, nullptr, HD, DM, HD / 2, (DM / 64) | (2 << 16)};
        k_gemm2<1, 128, 1, 6><<<(DM / 64) * (S_ / 128) * 2, 256, 6 * (128 * 128 + 8192), stream>>>(g1);
        k_res_ln<2><<<S_ / 4, 256, 0, stream>>>(Pk, x, hb, hn);
        GemmArgs g2{hn, Wg, Wu, nullptr, nullptr, act, nullptr, nullptr, DM, FF, DM, (FF / 64) | (1 << 16)};
        k_gemm2<2, 128, 2, 2><<<(FF / 64) * (S_ / 128), 256, 2 * (128 * 128 + 16384), stream>>>(g2);
        GemmArgs g3{act, Wd, nullptr, nullptr, hb, out, nullptr, nullptr, FF, DM, FF, (DM / 64) | (1 << 16)};
        k_gemm2<3, 64, 1, 8><<<(DM / 64) * (S_ / 64), 256, 8 * (64 * 128 + 8192), stream>>>(g3);
    }
}
```

```cpp
#include <hip/hip_runtime.h>
#include <hip/hip_bf16.h>

typedef unsigned short u16;
typedef __bf16 bf16x8 __attribute__((ext_vector_type(8)));
typedef __bf16 bf16x16 __attribute__((ext_vector_type(16)));
typedef float f32x4 __attribute__((ext_vector_type(4)));
typedef unsigned v4u __attribute__((ext_vector_type(4)));
typedef unsigned v2u __attribute__((ext_vector_type(2)));
typedef short v4s __attribute__((ext_vector_type(4)));
#define LAS __attribute__((address_space(3)))

constexpr int S_ = 2048, DM = 512, NH = 32, DH = 128, HD = NH * DH, MEMN = 8, SEG = 32, NSEG = 64, FF = 1792;

__device__ __forceinline__ float bf2f(u16 v) { return __uint_as_float(((unsigned)v) << 16); }
__device__ __forceinline__ u16 f2bf(float f) { return __builtin_bit_cast(u16, (__bf16)f); }
__device__ __forceinline__ float bfr(float f) { return bf2f(f2bf(f)); }
typedef float f32x2 __attribute__((ext_vector_type(2)));
typedef __bf16 bf16x2 __attribute__((ext_vector_type(2)));
__device__ __forceinline__ unsigned pack2(float a, float b) { f32x2 t = {a, b}; return __builtin_bit_cast(unsigned, __builtin_convertvector(t, bf16x2)); }
__device__ __forceinline__ float lo16f(unsigned w) { return __uint_as_float(w << 16); }
__device__ __forceinline__ float hi16f(unsigned w) { return __uint_as_float(w & 0xFFFF0000u); }
template <int CTRL> __device__ __forceinline__ float dppf(float x) {
    return __builtin_bit_cast(float, __builtin_amdgcn_mov_dpp(__builtin_bit_cast(int, x), CTRL, 0xf, 0xf, true));
}
__device__ __forceinline__ float xrow16_max(float x) {
    auto s = __builtin_amdgcn_permlane16_swap(__float_as_uint(x), __float_as_uint(x), false, false);
    x = fmaxf(__uint_as_float(s[0]), __uint_as_float(s[1]));
    auto t = __builtin_amdgcn_permlane32_swap(__float_as_uint(x), __float_as_uint(x), false, false);
    return fmaxf(__uint_as_float(t[0]), __uint_as_float(t[1]));
}
__device__ __forceinline__ float xrow16_sum(float x) {
    auto s = __builtin_amdgcn_permlane16_swap(__float_as_uint(x), __float_as_uint(x), false, false);
    x = __uint_as_float(s[0]) + __uint_as_float(s[1]);
    auto t = __builtin_amdgcn_permlane32_swap(__float_as_uint(x), __float_as_uint(x), false, false);
    return __uint_as_float(t[0]) + __uint_as_float(t[1]);
}
__device__ __forceinline__ unsigned umax3(unsigned a, unsigned b, unsigned c) { return max(max(a, b), c); }
__device__ __forceinline__ unsigned umax4(v4u v) { return max(umax3(v.x, v.y, v.z), v.w); }
__device__ __forceinline__ bool valid16(v4u v) { return umax4(v) != 0xFFFFFFFFu; }
#define SB0() __builtin_amdgcn_sched_barrier(0)
__device__ __forceinline__ void glds16(const void* gsrc, unsigned lds_dst) {
    unsigned keep;
    asm volatile("s_mov_b32 %0, m0\n\ts_mov_b32 m0, %2\n\ts_nop 0\n\tglobal_load_lds_dwordx4 %1, off\n\ts_mov_b32 m0, %0" : "=&s"(keep) : "v"(gsrc), "s"(lds_dst) : "memory");
}
__device__ __forceinline__ void glds16_sc1(const void* gsrc, unsigned lds_dst) {
    unsigned keep;
    asm volatile("s_mov_b32 %0, m0\n\ts_mov_b32 m0, %2\n\ts_nop 0\n\tglobal_load_lds_dwordx4 %1, off sc1\n\ts_mov_b32 m0, %0" : "=&s"(keep) : "v"(gsrc), "s"(lds_dst) : "memory");
}
__device__ __forceinline__ unsigned lds_addr(const void* p) { return (unsigned)(size_t)(LAS const unsigned char*)p; }
__device__ __forceinline__ unsigned nosent(unsigned w) { return min(w, 0xFFFFFFFEu); }
__device__ __forceinline__ v2u tr_read(const void* p) {
    return __builtin_bit_cast(v2u, __builtin_amdgcn_ds_read_tr16_b64_v4i16((LAS v4s*)p));
}
__device__ __forceinline__ bf16x8 mk8(v2u a, v2u b) { v4u t = {a.x, a.y, b.x, b.y}; return __builtin_bit_cast(bf16x8, t); }
__device__ __forceinline__ bf16x8 as8(v4u t) { return __builtin_bit_cast(bf16x8, t); }
__device__ __forceinline__ f32x4 mfma16(bf16x8 a, bf16x8 b, f32x4 c) { return __builtin_amdgcn_mfma_f32_16x16x32_bf16(a, b, c, 0, 0, 0); }

__device__ __forceinline__ float wave_sum(float s) {
    s += dppf<0xB1>(s); s += dppf<0x4E>(s); s += dppf<0x141>(s); s += dppf<0x128>(s);
    return xrow16_sum(s);
}
__device__ __forceinline__ v4u layernorm_vals(v4u raw) {
    float x[8];
#pragma unroll
    for (int i = 0; i < 4; ++i) { x[2 * i] = lo16f(raw[i]); x[2 * i + 1] = hi16f(raw[i]); }
    float s = ((x[0] + x[1]) + (x[2] + x[3])) + ((x[4] + x[5]) + (x[6] + x[7]));
    const float mean = wave_sum(s) * (1.0f / DM);
    float q = 0.f;
#pragma unroll
    for (int i = 0; i < 8; ++i) { float d = x[i] - mean; q += d * d; }
    q = wave_sum(q);
    const float mb = bfr(mean);
    const float vb = bfr(q * (1.0f / DM));
    const float den = bfr(sqrtf(bfr(vb + 1e-5f)));
    v4u outv;
#pragma unroll
    for (int i = 0; i < 4; ++i) {
        float a = bfr(x[2 * i] - mb) / den, b = bfr(x[2 * i + 1] - mb) / den;
        outv[i] = pack2(a, b);
    }
    return outv;
}
__device__ __forceinline__ void layernorm_row(const u16* __restrict__ src, u16* __restrict__ dst, int lane) {
    *(v4u*)(dst + lane * 8) = layernorm_vals(*(const v4u*)(src + lane * 8));
}

__global__ void __launch_bounds__(256) k_prep(const u16* __restrict__ x, u16* __restrict__ xn, const u16* __restrict__ mem0, u16* __restrict__ memx,
                                              v4u* __restrict__ sent, int n_sent16, int memx_off16, v4u* __restrict__ flags, int n_flag16) {
    const int wave = threadIdx.x >> 6, lane = threadIdx.x & 63;
    const int row = blockIdx.x * 4 + wave;
    if (row < S_) layernorm_row(x + (size_t)row * DM, xn + (size_t)row * DM, lane);
    const v4u ones = {0xFFFFFFFFu, 0xFFFFFFFFu, 0xFFFFFFFFu, 0xFFFFFFFFu};
    const int gt = blockIdx.x * 256 + threadIdx.x, nt = gridDim.x * 256;
    for (int i = gt; i < n_sent16; i += nt)
        if (i < memx_off16 || i >= memx_off16 + 512) sent[i] = ones;
    for (int i = gt; i < n_flag16; i += nt) flags[i] = (v4u){0u, 0u, 0u, 0u};
    if (blockIdx.x == 0) {
        for (int i = threadIdx.x; i < MEMN * DM; i += 256) {
            int k = i >> 3, r = i & 7;
            u16 v = mem0[r * DM + k];
            if (v == 0xFFFFu) v = 0x7FFFu;
            memx[i] = v;
        }
    }
}

struct GemmArgs {
    const u16* A; const u16* B0; const u16* B1; const u16* B2; const u16* res;
    u16* C0; u16* C1; u16* C2;
    int lda, ldb, K, pad;
};

__device__ __forceinline__ unsigned offB(unsigned row, unsigned ch) { return 256u * row + 16u * (ch ^ (((row & 3) << 2) | ((row >> 2) & 3))); }
__device__ __forceinline__ unsigned offA(unsigned row, unsigned ch) { return 128u * row + 16u * (ch ^ (row & 7)); }

__device__ __forceinline__ unsigned fB(unsigned row) { return (((row >> 1) & 1) | (((row >> 3) & 1) << 1)) << 1; }
template <int MODE, int BM, int NB, int NST, bool SC1OUT>
__device__ __forceinline__ void gemm_body(const GemmArgs& g, unsigned char* sm, int bm, int bn, int bz) {
    constexpr int MT = BM / 32;
    constexpr int ABYTES = BM * 128, STAGE = ABYTES + NB * 8192;
    constexpr int LA = BM / 32, LPS = LA + 2 * NB;
    const int tid = threadIdx.x, lane = tid & 63;
    const int wave = __builtin_amdgcn_readfirstlane(tid >> 6);
    const int wm = wave >> 1, wn = wave & 1;
    const int kbase = bz * g.K;
    const u16* Asrc[LA];
    const u16* Bsrc[NB][2];
#pragma unroll
    for (int i = 0; i < LA; ++i) { int q = tid + 256 * i; int row = q >> 3, ch = (q & 7) ^ (row & 7); Asrc[i] = g.A + (size_t)(bm + row) * g.lda + kbase + ch * 8; }
#pragma unroll
    for (int n = 0; n < NB; ++n)
#pragma unroll
        for (int i = 0; i < 2; ++i) { int q = tid + 256 * i; int row = q >> 3, ch = (q & 7) ^ fB(row); Bsrc[n][i] = (n == 0 ? g.B0 : g.B1) + (size_t)(kbase + row) * g.ldb + bn + ch * 8; }
    const unsigned sm_base = __builtin_amdgcn_readfirstlane(lds_addr(sm)) + wave * 1024;
    auto stage = [&](int kt) {
        const unsigned base = sm_base + (kt % NST) * STAGE;
#pragma unroll
        for (int i = 0; i < LA; ++i) glds16(Asrc[i] + kt * 64, base + i * 4096);
#pragma unroll
        for (int n = 0; n < NB; ++n)
#pragma unroll
            for (int i = 0; i < 2; ++i) glds16(Bsrc[n][i] + (size_t)kt * 64 * g.ldb, base + ABYTES + n * 8192 + i * 4096);
    };
    f32x4 acc[NB][MT][2];
#pragma unroll
    for (int n = 0; n < NB; ++n)
#pragma unroll
        for (int i = 0; i < MT; ++i) { acc[n][i][0] = (f32x4){0.f, 0.f, 0.f, 0.f}; acc[n][i][1] = acc[n][i][0]; }
    const int nk = g.K / 64;
#pragma unroll
    for (int t = 0; t < NST - 1; ++t) stage(t);
    const int G = lane >> 4, l15 = lane & 15, tq = l15 >> 2, tp = lane & 3;
    for (int kt = 0; kt < nk; ++kt) {
        if (kt + NST - 1 <= nk) asm volatile("s_waitcnt vmcnt(%0)" :: "i"((NST - 2) * LPS) : "memory");
        else asm volatile("s_waitcnt vmcnt(0)" ::: "memory");
        asm volatile("s_waitcnt lgkmcnt(0)" ::: "memory");
        __builtin_amdgcn_s_barrier();
        if (kt + NST - 1 < nk) stage(kt + NST - 1);
        const unsigned char* sA = sm + (kt % NST) * STAGE;
        const unsigned char* sB = sA + ABYTES;
#pragma unroll
        for (int ks = 0; ks < 2; ++ks) {
            bf16x8 af[MT], bq[NB][2];
#pragma unroll
            for (int i = 0; i < MT; ++i) af[i] = as8(*(const v4u*)(sA + offA(wm * (BM / 2) + i * 16 + l15, ks * 4 + G)));
#pragma unroll
            for (int n = 0; n < NB; ++n)
#pragma unroll
                for (int j = 0; j < 2; ++j) {
                    const unsigned r0 = 32 * ks + 8 * G + tq, r1 = r0 + 4, ch = (wn * 2 + j) * 2 + (tp >> 1);
                    v2u lo = tr_read(sB + n * 8192 + r0 * 128 + ((ch ^ fB(r0)) * 16) + 8 * (tp & 1));
                    v2u hi = tr_read(sB + n * 8192 + r1 * 128 + ((ch ^ fB(r1)) * 16) + 8 * (tp & 1));
                    bq[n][j] = mk8(lo, hi);
                }
#pragma unroll
            for (int n = 0; n < NB; ++n)
#pragma unroll
                for (int i = 0; i < MT; ++i)
#pragma unroll
                    for (int j = 0; j < 2; ++j) acc[n][i][j] = mfma16(bq[n][j], af[i], acc[n][i][j]);
        }
    }
#pragma unroll
    for (int i = 0; i < MT; ++i)
#pragma unroll
        for (int j = 0; j < 2; ++j) {
            const int row = bm + wm * (BM / 2) + i * 16 + l15;
            const int col = bn + (wn * 2 + j) * 16 + 4 * G;
            if (MODE == 1) {
                float* P = (float*)g.C0 + ((size_t)bz * S_ + row) * DM + col;
                *(f32x4*)P = acc[0][i][j];
            } else if (MODE == 2) {
                unsigned g0 = pack2(acc[0][i][j][0], acc[0][i][j][1]), g1 = pack2(acc[0][i][j][2], acc[0][i][j][3]);
                unsigned u0 = pack2(acc[NB - 1][i][j][0], acc[NB - 1][i][j][1]), u1 = pack2(acc[NB - 1][i][j][2], acc[NB - 1][i][j][3]);
                v2u v = {pack2(lo16f(g0) * lo16f(u0), hi16f(g0) * hi16f(u0)), pack2(lo16f(g1) * lo16f(u1), hi16f(g1) * hi16f(u1))};
                if (SC1OUT) __hip_atomic_store((unsigned long long*)(g.C0 + (size_t)row * FF + col), __builtin_bit_cast(unsigned long long, v), __ATOMIC_RELAXED, __HIP_MEMORY_SCOPE_AGENT);
                else *(v2u*)(g.C0 + (size_t)row * FF + col) = v;
            } else {
                v2u r = *(const v2u*)(g.res + (size_t)row * DM + col);
                unsigned a0 = pack2(acc[0][i][j][0], acc[0][i][j][1]), a1 = pack2(acc[0][i][j][2], acc[0][i][j][3]);
                v2u v = {pack2(lo16f(r.x) + lo16f(a0), hi16f(r.x) + hi16f(a0)), pack2(lo16f(r.y) + lo16f(a1), hi16f(r.y) + hi16f(a1))};
                if (SC1OUT) __hip_atomic_store((unsigned long long*)(g.C0 + (size_t)row * DM + col), __builtin_bit_cast(unsigned long long, v), __ATOMIC_RELAXED, __HIP_MEMORY_SCOPE_AGENT);
                else *(v2u*)(g.C0 + (size_t)row * DM + col) = v;
            }
        }
}

template <int MODE, int BM, int NB, int NST>
__global__ void __launch_bounds__(256) k_gemm2(GemmArgs g, int row0) {
    extern __shared__ __attribute__((aligned(16))) unsigned char sm[];
    const int ncol = g.pad & 0xFFFF, nz = g.pad >> 16;
    const int per_xcd = gridDim.x >> 3;
    const int lin = (blockIdx.x & 7) * per_xcd + (blockIdx.x >> 3);
    const int panel = lin / ncol;
    gemm_body<MODE, BM, NB, NST, false>(g, sm, row0 + (panel / nz) * BM, (lin % ncol) * 64, panel % nz);
}

template <int NZ>
__global__ void __launch_bounds__(256) k_res_ln(const float* __restrict__ P, const u16* __restrict__ x, u16* __restrict__ hb, u16* __restrict__ hn, int row0) {
    const int wave = threadIdx.x >> 6, lane = threadIdx.x & 63;
    const int row = row0 + blockIdx.x * 4 + wave;
    f32x4 s0 = *(const f32x4*)(P + (size_t)row * DM + lane * 8), s1 = *(const f32x4*)(P + (size_t)row * DM + lane * 8 + 4);
#pragma unroll
    for (int z = 1; z < NZ; ++z) { s0 += *(const f32x4*)(P + ((size_t)z * S_ + row) * DM + lane * 8); s1 += *(const f32x4*)(P + ((size_t)z * S_ + row) * DM + lane * 8 + 4); }
    v4u raw = *(const v4u*)(x + (size_t)row * DM + lane * 8);
    unsigned a0 = pack2(s0[0], s0[1]), a1 = pack2(s0[2], s0[3]), a2 = pack2(s1[0], s1[1]), a3 = pack2(s1[2], s1[3]);
    v4u hv = {pack2(lo16f(raw.x) + lo16f(a0), hi16f(raw.x) + hi16f(a0)), pack2(lo16f(raw.y) + lo16f(a1), hi16f(raw.y) + hi16f(a1)),
              pack2(lo16f(raw.z) + lo16f(a2), hi16f(raw.z) + hi16f(a2)), pack2(lo16f(raw.w) + lo16f(a3), hi16f(raw.w) + hi16f(a3))};
    *(v4u*)(hb + (size_t)row * DM + lane * 8) = hv;
    *(v4u*)(hn + (size_t)row * DM + lane * 8) = layernorm_vals(hv);
}

#define KX_DMA 0
#define XDELAY 0
#define YDELAY 0
constexpr int NX = 64;
constexpr int MEMX_STEP = 8192;
constexpr int PART_BUF = 64 * 64 * 128;
constexpr unsigned SPIN_CAP = 1u << 22;

#define WSTAMP() do {} while (0)
#define STAMP(i) do {} while (0)
#define DIAG_DECL
#define DIAG_END
struct RecArgs {
    const u16* Wo;
    u16* K; u16* VT; u16* Q; u16* O;
    u16* KM; u16* VMT;
    unsigned char* memx; unsigned char* part;
    const u16* xn; const u16* Wq; const u16* Wk; const u16* Wv;
    unsigned* flags;
    const u16* x; const u16* Wg; const u16* Wu; const u16* Wd;
    u16* hb; u16* hn; u16* act; u16* out;
};
constexpr int RB_IN = 12;
constexpr int CNT_BASE = 16 * 96;
constexpr int NWORK = 128;
constexpr int FLAG_STRIDE = 16;

__device__ __forceinline__ void wait_flag(const unsigned* f) {
    unsigned spins = 0;
    while (__builtin_amdgcn_readfirstlane(__hip_atomic_load(f, __ATOMIC_RELAXED, __HIP_MEMORY_SCOPE_AGENT)) == 0u) {
        __builtin_amdgcn_s_sleep(2);
        if (++spins > SPIN_CAP) break;
    }
    asm volatile("" ::: "memory");
}

__device__ __forceinline__ void wait_count_acquire(const unsigned* c, unsigned target) {
    if (threadIdx.x < 64) {
        unsigned spins = 0;
        while (__builtin_amdgcn_readfirstlane(__hip_atomic_load(c, __ATOMIC_RELAXED, __HIP_MEMORY_SCOPE_AGENT)) < target) {
            __builtin_amdgcn_s_sleep(32);
            if (++spins > SPIN_CAP) break;
        }
        __builtin_amdgcn_fence(__ATOMIC_ACQUIRE, "agent");
        asm volatile("s_waitcnt vmcnt(0)" ::: "memory");
    }
    __syncthreads();
}
__device__ __forceinline__ void signal_count(unsigned* c) {
    asm volatile("s_waitcnt vmcnt(0)" ::: "memory");
    __syncthreads();
    if (threadIdx.x == 0) __hip_atomic_fetch_add(c, 1u, __ATOMIC_RELAXED, __HIP_MEMORY_SCOPE_AGENT);
}

template <bool ISV>
__device__ __forceinline__ void proj_col(const RecArgs& a, unsigned char* lds, int c) {
    constexpr int NST = 8;
    const int tid = threadIdx.x, lane = tid & 63;
    const int wave = __builtin_amdgcn_readfirstlane(tid >> 6);
    const int wm = wave >> 1, wn = wave & 1;
    const int mat = c >> 5, head = c & 31;
    const int G = lane >> 4, l15 = lane & 15, tq = l15 >> 2, tp = lane & 3;
    const unsigned sbase = __builtin_amdgcn_readfirstlane(lds_addr(lds)) + wave * 1024;
    bf16x8 bw[4][16];
    {
        const u16* Bp = (mat == 0 ? a.Wk : (mat == 1 ? a.Wv : a.Wq)) + head * DH;
#pragma unroll
        for (int kt = 0; kt < 8; ++kt)
#pragma unroll
            for (int i = 0; i < 4; ++i) {
                const int q = tid + 256 * i, r = q >> 4;
                glds16(Bp + (size_t)(kt * 64 + r) * HD + (((q & 15) ^ (((r & 3) << 2) | ((r >> 2) & 3))) * 8), sbase + kt * 16384 + i * 4096);
            }
        asm volatile("s_waitcnt vmcnt(0)" ::: "memory");
        __builtin_amdgcn_s_barrier();
#pragma unroll
        for (int t = 0; t < 16; ++t)
#pragma unroll
            for (int j = 0; j < 4; ++j) {
                const unsigned char* sB = lds + (t >> 1) * 16384;
                const int cc = wn * 4 + j, ks = t & 1;
                v2u lo = tr_read(sB + offB(32 * ks + 8 * G + tq, 2 * cc + (tp >> 1)) + 8 * (tp & 1));
                v2u hi = tr_read(sB + offB(32 * ks + 8 * G + 4 + tq, 2 * cc + (tp >> 1)) + 8 * (tp & 1));
                bw[j][t] = mk8(lo, hi);
            }
        asm volatile("s_waitcnt lgkmcnt(0)" ::: "memory");
        __builtin_amdgcn_s_barrier();
    }
    const u16* asrc[4];
#pragma unroll
    for (int i = 0; i < 4; ++i) { const int q = tid + 256 * i; asrc[i] = a.xn + (size_t)(q >> 3) * DM + (((q & 7) ^ ((q >> 3) & 7)) * 8); }
    auto stage = [&](int n) {
        const unsigned d = sbase + (n & (NST - 1)) * 16384;
        const size_t off = (size_t)(n >> 3) * 128 * DM + (n & 7) * 64;
#pragma unroll
        for (int i = 0; i < 4; ++i) glds16(asrc[i] + off, d + i * 4096);
    };
#pragma unroll
    for (int n = 0; n < NST - 1; ++n) stage(n);
    unsigned char* img = lds + 131072;
#pragma unroll 1
    for (int rb = 0; rb < 16; ++rb) {
        f32x4 acc[4][4];
#pragma unroll
        for (int i = 0; i < 4; ++i)
#pragma unroll
            for (int j = 0; j < 4; ++j) acc[i][j] = (f32x4){0.f, 0.f, 0.f, 0.f};
#pragma unroll
        for (int kt = 0; kt < 8; ++kt) {
            const int n = rb * 8 + kt;
            if (n + NST - 1 > 128) asm volatile("s_waitcnt vmcnt(0)" ::: "memory");
            else if (kt < 7 && rb > 0) asm volatile("s_waitcnt vmcnt(32)" ::: "memory");
            else asm volatile("s_waitcnt vmcnt(24)" ::: "memory");
            asm volatile("s_waitcnt lgkmcnt(0)" ::: "memory");
            __builtin_amdgcn_s_barrier();
            if (kt == 7 && rb >= 2 && tid == 0) __hip_atomic_store(a.flags + ((rb - 1) * 96 + c) * FLAG_STRIDE, 1u, __ATOMIC_RELAXED, __HIP_MEMORY_SCOPE_AGENT);
            if (n + NST - 1 < 128) stage(n + NST - 1);
            const unsigned char* sA = lds + kt * 16384;
#pragma unroll
            for (int ks = 0; ks < 2; ++ks) {
                bf16x8 af[4];
#pragma unroll
                for (int i = 0; i < 4; ++i) af[i] = as8(*(const v4u*)(sA + offA(wm * 64 + i * 16 + l15, ks * 4 + G)));
#pragma unroll
                for (int i = 0; i < 4; ++i)
#pragma unroll
                    for (int j = 0; j < 4; ++j) acc[i][j] = ISV ? mfma16(af[i], bw[j][kt * 2 + ks], acc[i][j]) : mfma16(bw[j][kt * 2 + ks], af[i], acc[i][j]);
            }
        }
#pragma unroll
        for (int i = 0; i < 4; ++i)
#pragma unroll
            for (int j = 0; j < 4; ++j) {
                v2u v = {pack2(acc[i][j][0], acc[i][j][1]), pack2(acc[i][j][2], acc[i][j][3])};
                if (ISV) {
                    const int e = (wn * 4 + j) * 16 + l15, row0 = wm * 64 + i * 16 + 4 * G;
                    const int line = (row0 >> 5) * 128 + e;
                    *(v2u*)(img + line * 64 + ((((row0 & 31) >> 2) ^ (e & 7)) * 8)) = v;
                } else {
                    const int row = wm * 64 + i * 16 + l15, col0 = (wn * 4 + j) * 16 + 4 * G;
                    *(v2u*)(img + row * 256 + (((col0 >> 3) ^ (row & 15)) * 16) + ((col0 & 4) * 2)) = v;
                }
            }
        asm volatile("s_waitcnt lgkmcnt(0)" ::: "memory");
        __builtin_amdgcn_s_barrier();
        if (ISV) {
            const __amdgpu_buffer_rsrc_t r = __builtin_amdgcn_make_buffer_rsrc(a.VT, 0, S_ * HD * 2, 0x00020000);
#pragma unroll
            for (int i = 0; i < 8; ++i) {
                const int q = tid + 256 * i;
                const int line = q >> 2, cq = q & 3, e = line & 127;
                v2u x0 = *(const v2u*)(img + line * 64 + (((2 * cq) ^ (e & 7)) * 8));
                v2u x1 = *(const v2u*)(img + line * 64 + (((2 * cq + 1) ^ (e & 7)) * 8));
                v4u v = {x0.x, x0.y, x1.x, x1.y};
                const int seg = rb * 4 + (line >> 7);
                __builtin_amdgcn_raw_buffer_store_b128(v, r, (unsigned)((((seg * NH + head) * DH + e) * 32 + cq * 8) * 2), 0, 16);
            }
        } else {
            const __amdgpu_buffer_rsrc_t r = __builtin_amdgcn_make_buffer_rsrc(mat == 0 ? a.K : a.Q, 0, S_ * HD * 2, 0x00020000);
#pragma unroll
            for (int i = 0; i < 8; ++i) {
                const int q = tid + 256 * i;
                const int row = q >> 4, ch = q & 15;
                v4u v = *(const v4u*)(img + row * 256 + ((ch ^ (row & 15)) * 16));
                __builtin_amdgcn_raw_buffer_store_b128(v, r, (unsigned)(((rb * 128 + row) * HD + head * DH + ch * 8) * 2), 0, 16);
            }
        }
        if (rb == 0 || rb == 15) {
            asm volatile("s_waitcnt vmcnt(0) lgkmcnt(0)" ::: "memory");
            __builtin_amdgcn_s_barrier();
            if (tid == 0) __hip_atomic_store(a.flags + (rb * 96 + c) * FLAG_STRIDE, 1u, __ATOMIC_RELAXED, __HIP_MEMORY_SCOPE_AGENT);
        }
    }
    asm volatile("s_waitcnt vmcnt(0)" ::: "memory");
    __syncthreads();
}

__device__ __forceinline__ void attn_unit(const RecArgs& a, int seg, int h, int lane) {
    const int G = lane >> 4, l15 = lane & 15;
    const float scale = 0.08838834764831845f;
    const __amdgpu_buffer_rsrc_t rq = __builtin_amdgcn_make_buffer_rsrc(a.Q, 0, S_ * HD * 2, 0x00020000);
    const __amdgpu_buffer_rsrc_t rk = __builtin_amdgcn_make_buffer_rsrc(a.K, 0, S_ * HD * 2, 0x00020000);
    const __amdgpu_buffer_rsrc_t rv = __builtin_amdgcn_make_buffer_rsrc(a.VT, 0, S_ * HD * 2, 0x00020000);
    const __amdgpu_buffer_rsrc_t rkm = __builtin_amdgcn_make_buffer_rsrc(a.KM, 0, NSEG * 8 * HD * 2, 0x00020000);
    const __amdgpu_buffer_rsrc_t rvm = __builtin_amdgcn_make_buffer_rsrc(a.VMT, 0, NSEG * 8 * HD * 2, 0x00020000);
    const int rb = seg >> 2;
    wait_flag(a.flags + (rb * 96 + h) * FLAG_STRIDE);
    wait_flag(a.flags + (rb * 96 + 32 + h) * FLAG_STRIDE);
    wait_flag(a.flags + (rb * 96 + 64 + h) * FLAG_STRIDE);
    v4u k1[4], k2[4], q0[4], q1[4];
    v2u v1[8], v2[8];
#pragma unroll
    for (int ks = 0; ks < 4; ++ks) {
        const unsigned eo = (unsigned)(h * DH + ks * 32 + 8 * G) * 2;
        k1[ks] = __builtin_amdgcn_raw_buffer_load_b128(rk, (unsigned)(seg * 32 + l15) * (HD * 2) + eo, 0, 16);
        k2[ks] = __builtin_amdgcn_raw_buffer_load_b128(rk, (unsigned)(seg * 32 + 16 + l15) * (HD * 2) + eo, 0, 16);
        q0[ks] = __builtin_amdgcn_raw_buffer_load_b128(rq, (unsigned)(seg * 32 + l15) * (HD * 2) + eo, 0, 16);
        q1[ks] = __builtin_amdgcn_raw_buffer_load_b128(rq, (unsigned)(seg * 32 + 16 + l15) * (HD * 2) + eo, 0, 16);
    }
#pragma unroll
    for (int et = 0; et < 8; ++et) {
        const unsigned vo = (unsigned)(((seg * NH + h) * DH + et * 16 + l15) * 32 + 4 * G) * 2;
        v1[et] = __builtin_amdgcn_raw_buffer_load_b64(rv, vo, 0, 16);
        v2[et] = __builtin_amdgcn_raw_buffer_load_b64(rv, vo + 32, 0, 16);
    }
    v4u k0[4]; v2u vm[8];
    {
        unsigned spins = 0;
        const unsigned* probe = (const unsigned*)(a.VMT + ((size_t)(seg * NH + h) * DH + 127) * 8);
        while (__builtin_amdgcn_readfirstlane(__hip_atomic_load(probe, __ATOMIC_RELAXED, __HIP_MEMORY_SCOPE_AGENT)) == 0xFFFFFFFFu) {
            __builtin_amdgcn_s_sleep(100);
            if (++spins > SPIN_CAP) break;
        }
        for (;;) {
            unsigned mx = 0;
#pragma unroll
            for (int ks = 0; ks < 4; ++ks) {
                k0[ks] = __builtin_amdgcn_raw_buffer_load_b128(rkm, (unsigned)((seg * 8 + (l15 & 7)) * HD + h * DH + ks * 32 + 8 * G) * 2, 0, 16);
            }
#pragma unroll
            for (int et = 0; et < 8; ++et) vm[et] = __builtin_amdgcn_raw_buffer_load_b64(rvm, (unsigned)(((seg * NH + h) * DH + et * 16 + l15) * 8 + 4 * (G & 1)) * 2, 0, 16);
#pragma unroll
            for (int ks = 0; ks < 4; ++ks) mx = max(mx, umax4(k0[ks]));
#pragma unroll
            for (int et = 0; et < 8; ++et) mx = umax3(mx, vm[et].x, vm[et].y);
            asm volatile("" ::: "memory");
            if (__all(mx != 0xFFFFFFFFu)) break;
            __builtin_amdgcn_s_sleep(16);
            if (++spins > SPIN_CAP) break;
        }
    }
    f32x4 st[3][2];
#pragma unroll
    for (int i = 0; i < 3; ++i) { st[i][0] = (f32x4){0.f, 0.f, 0.f, 0.f}; st[i][1] = st[i][0]; }
#pragma unroll
    for (int ks = 0; ks < 4; ++ks) {
        st[0][0] = mfma16(as8(k0[ks]), as8(q0[ks]), st[0][0]); st[0][1] = mfma16(as8(k0[ks]), as8(q1[ks]), st[0][1]);
        st[1][0] = mfma16(as8(k1[ks]), as8(q0[ks]), st[1][0]); st[1][1] = mfma16(as8(k1[ks]), as8(q1[ks]), st[1][1]);
        st[2][0] = mfma16(as8(k2[ks]), as8(q0[ks]), st[2][0]); st[2][1] = mfma16(as8(k2[ks]), as8(q1[ks]), st[2][1]);
    }
    bf16x8 pf[2][2];
#pragma unroll
    for (int qt = 0; qt < 2; ++qt) {
        const int t = qt * 16 + l15;
        float v[3][4];
        float m = -3.0e38f;
#pragma unroll
        for (int i = 0; i < 3; ++i)
#pragma unroll
            for (int r = 0; r < 4; ++r) {
                float sc = bfr(bfr(st[i][qt][r]) * scale);
                bool ok;
                if (i == 0) ok = (G < 2); else ok = ((i - 1) * 16 + 4 * G + r) <= t;
                bool exist = (i != 0) || (G < 2);
                sc = ok ? sc : bfr(-1.0e9f);
                v[i][r] = exist ? sc : -3.0e38f;
                m = fmaxf(m, v[i][r]);
            }
        m = xrow16_max(m);
        float sum = 0.f;
#pragma unroll
        for (int i = 0; i < 3; ++i)
#pragma unroll
            for (int r = 0; r < 4; ++r) { bool exist = (i != 0) || (G < 2); v[i][r] = exist ? __expf(v[i][r] - m) : 0.f; sum += v[i][r]; }
        sum = xrow16_sum(sum);
        const float inv = 1.0f / sum;
        v4u p0 = {pack2(v[0][0] * inv, v[0][1] * inv), pack2(v[0][2] * inv, v[0][3] * inv), pack2(v[1][0] * inv, v[1][1] * inv), pack2(v[1][2] * inv, v[1][3] * inv)};
        v4u p1 = {pack2(v[2][0] * inv, v[2][1] * inv), pack2(v[2][2] * inv, v[2][3] * inv), 0u, 0u};
        pf[qt][0] = as8(p0); pf[qt][1] = as8(p1);
    }
#pragma unroll
    for (int et = 0; et < 8; ++et) {
        bf16x8 a0 = mk8(vm[et], v1[et]);
        bf16x8 a1 = mk8(v2[et], (v2u){0u, 0u});
#pragma unroll
        for (int qt = 0; qt < 2; ++qt) {
            f32x4 o = (f32x4){0.f, 0.f, 0.f, 0.f};
            o = mfma16(a0, pf[qt][0], o);
            o = mfma16(a1, pf[qt][1], o);
            v2u ov = {pack2(o[0], o[1]), pack2(o[2], o[3])};
            __hip_atomic_store((unsigned long long*)(a.O + ((size_t)seg * 32 + qt * 16 + l15) * HD + h * DH + et * 16 + 4 * G), __builtin_bit_cast(unsigned long long, ov), __ATOMIC_RELAXED, __HIP_MEMORY_SCOPE_AGENT);
        }
    }
}

constexpr int L_WO = 0;
constexpr int L_MEM = 65536;
constexpr int L_T = L_MEM + 8192;
constexpr int L_VM = L_T + 4096;
constexpr int L_KX = L_VM + 2048;
constexpr int L_VX = L_KX + 8192;
constexpr int L_STG = 96 * 1024;
constexpr int L_TOTAL = 160 * 1024;

__global__ void __launch_bounds__(256, 1) k_rec(RecArgs a) {
    extern __shared__ __attribute__((aligned(16))) unsigned char lds[];
    const int tid = threadIdx.x, lane = tid & 63;
    const int wave = __builtin_amdgcn_readfirstlane(tid >> 6);
    const int G = lane >> 4, l15 = lane & 15;
    const __amdgpu_buffer_rsrc_t rmem = __builtin_amdgcn_make_buffer_rsrc(a.memx, 0, (NSEG + 1) * MEMX_STEP, 0x00020000);
    const __amdgpu_buffer_rsrc_t rpart = __builtin_amdgcn_make_buffer_rsrc(a.part, 0, 2 * PART_BUF, 0x00020000);

    if (blockIdx.x >= 2 * NX) {
        const int w = blockIdx.x - 2 * NX;
        int wsn_ = 0; (void)wsn_;
        WSTAMP();
        if (w >= 32) {
            const int c = w - 32;
            if ((c >> 5) == 1) proj_col<true>(a, lds, c); else proj_col<false>(a, lds, c);
        }
        WSTAMP();
        if (w < 32) {
            for (int rb = 0; rb < 16; ++rb) {
                attn_unit(a, rb * 4 + (w >> 3), (w & 7) * 4 + wave, lane);
                signal_count(a.flags + (CNT_BASE + rb * 4) * FLAG_STRIDE);
                WSTAMP();
            }
        } else if (w < 80) {
            const int t = (w - 32) & 15;
            for (int rb = (w - 32) >> 4; rb < RB_IN; rb += 3) {
                unsigned* cnt = a.flags + (CNT_BASE + rb * 4) * FLAG_STRIDE;
                wait_count_acquire(cnt, 32);
                WSTAMP();
                GemmArgs g{a.O, a.Wo, nullptr, nullptr, a.x, a.hb, nullptr, nullptr, HD, DM, HD, 0};
                gemm_body<3, 64, 1, 8, true>(g, lds, rb * 128 + (t >> 3) * 64, (t & 7) * 64, 0);
                signal_count(cnt + FLAG_STRIDE);
                WSTAMP();
                wait_count_acquire(cnt + FLAG_STRIDE, 16);
                const __amdgpu_buffer_rsrc_t rh = __builtin_amdgcn_make_buffer_rsrc(a.hn, 0, S_ * DM * 2, 0x00020000);
                const int row0 = rb * 128 + t * 8 + wave * 2;
                v4u h0 = *(const v4u*)(a.hb + (size_t)row0 * DM + lane * 8), h1 = *(const v4u*)(a.hb + (size_t)(row0 + 1) * DM + lane * 8);
                __builtin_amdgcn_raw_buffer_store_b128(layernorm_vals(h0), rh, (unsigned)(row0 * DM + lane * 8) * 2, 0, 16);
                __builtin_amdgcn_raw_buffer_store_b128(layernorm_vals(h1), rh, (unsigned)((row0 + 1) * DM + lane * 8) * 2, 0, 16);
                signal_count(cnt + 2 * FLAG_STRIDE);
            }
        } else if (w < 112) {
            for (int u = w - 80; u < RB_IN * 28; u += 32) {
                const int rb = u / 28;
                unsigned* cnt = a.flags + (CNT_BASE + rb * 4) * FLAG_STRIDE;
                wait_count_acquire(cnt + 2 * FLAG_STRIDE, 16);
                WSTAMP();
                GemmArgs g{a.hn, a.Wg, a.Wu, nullptr, nullptr, a.act, nullptr, nullptr, DM, FF, DM, 0};
                gemm_body<2, 128, 2, 4, true>(g, lds, rb * 128, (u % 28) * 64, 0);
                signal_count(cnt + 3 * FLAG_STRIDE);
                WSTAMP();
            }
        } else {
            for (int rb = (w - 112) >> 3; rb < RB_IN; rb += 2) {
                unsigned* cnt = a.flags + (CNT_BASE + rb * 4) * FLAG_STRIDE;
                wait_count_acquire(cnt + 3 * FLAG_STRIDE, 28);
                WSTAMP();
                GemmArgs g{a.act, a.Wd, nullptr, nullptr, a.hb, a.out, nullptr, nullptr, FF, DM, FF, 0};
                gemm_body<3, 128, 1, 6, false>(g, lds, rb * 128, (w & 7) * 64, 0);
                __syncthreads();
                WSTAMP();
            }
        }
        return;
    }
    if (blockIdx.x >= NX) {
        if (wave != 0) return;
        const int y = blockIdx.x - NX;
        DIAG_DECL
        unsigned long long typub = 0;
        bf16x8 sel;
#pragma unroll
        for (int j = 0; j < 8; ++j) sel[j] = (l15 == j) ? (__bf16)1.0f : (__bf16)0.0f;
        for (int s = 0; s < NSEG; ++s) {
            const unsigned base = (s & 1) * PART_BUF + y * 8192 + lane * 16;
            v4u m[8];
            unsigned spins = 0;
            for (;;) {
#pragma unroll
                for (int t = 0; t < 8; ++t) m[t] = __builtin_amdgcn_raw_buffer_load_b128(rpart, base + t * 1024, 0, 16);
                unsigned mx = 0;
#pragma unroll
                for (int t = 0; t < 8; ++t) mx = umax3(mx, umax3(m[t].x, m[t].y, m[t].z), m[t].w);
                asm volatile("" ::: "memory");
                if (__all(mx != 0xFFFFFFFFu)) break;
                if (++spins > SPIN_CAP) break;
            }
            STAMP(0);
            f32x4 acc0 = (f32x4){0.f, 0.f, 0.f, 0.f}, acc1 = acc0;
#pragma unroll
            for (int t = 0; t < 8; t += 2) { acc0 = mfma16(sel, as8(m[t]), acc0); acc1 = mfma16(sel, as8(m[t + 1]), acc1); }
            acc0 += acc1;
#pragma unroll
            for (int r = 0; r < 4; ++r) acc0[r] += dppf<0x128>(acc0[r]);
            if (lane < 32 && l15 < 8) {
                v2u v = {nosent(pack2(acc0[0], acc0[1])), nosent(pack2(acc0[2], acc0[3]))};
                __builtin_amdgcn_raw_buffer_store_b64(v, rmem, (s + 1) * MEMX_STEP + y * 128 + l15 * 16 + G * 8, 0, 16);
            }
            STAMP(1);
            const v4u sent = {0xFFFFFFFFu, 0xFFFFFFFFu, 0xFFFFFFFFu, 0xFFFFFFFFu};
#pragma unroll
            for (int t = 0; t < 8; ++t) __builtin_amdgcn_raw_buffer_store_b128(sent, rpart, base + t * 1024, 0, 16);
            asm volatile("s_waitcnt vmcnt(0)" ::: "memory");
        }
        DIAG_END;
        return;
    }

    const int b = blockIdx.x, h = b >> 1, g = b & 1;
    bf16x16 wqk[2][16];
    bf16x8 wv[16];
    {
        const int tq_ = l15 >> 2, tp_ = lane & 3;
        const unsigned stg = __builtin_amdgcn_readfirstlane(lds_addr(lds)) + wave * 1024;
        int srow[4], sch[4];
#pragma unroll
        for (int i = 0; i < 4; ++i) { const int q = tid + 256 * i; srow[i] = q >> 4; sch[i] = ((q & 15) ^ (((srow[i] & 3) << 2) | ((srow[i] >> 2) & 3))) * 8; }
        auto issue = [&](int c) {
            const u16* s0; const u16* s1; int ld;
            if (c < 8) { s0 = a.Wq + (size_t)(64 * c) * HD + h * DH; s1 = a.Wk + (size_t)(64 * c) * HD + h * DH; ld = HD; }
            else if (c < 12) { s0 = a.Wv + (size_t)(128 * (c - 8)) * HD + h * DH; s1 = s0 + (size_t)64 * HD; ld = HD; }
            else { s0 = a.Wo + (size_t)(h * DH + 64 * g) * DM + 256 * (c - 12); s1 = s0 + 128; ld = DM; }
            const unsigned dst = stg + (((c & 3) < 2) ? (unsigned)(L_STG + (c & 1) * 32768) : (unsigned)(L_WO + (c & 1) * 32768));
#pragma unroll
            for (int i = 0; i < 4; ++i) glds16(s0 + (size_t)srow[i] * ld + sch[i], dst + i * 4096);
#pragma unroll
            for (int i = 0; i < 4; ++i) glds16(s1 + (size_t)srow[i] * ld + sch[i], dst + 16384 + i * 4096);
        };
        issue(0); issue(1); issue(2);
#pragma unroll
        for (int c = 0; c < 14; ++c) {
            if (c + 3 < 14) { issue(c + 3); asm volatile("s_waitcnt vmcnt(24)" ::: "memory"); }
            else if (c + 3 == 14) asm volatile("s_waitcnt vmcnt(16)" ::: "memory");
            else if (c + 2 == 14) asm volatile("s_waitcnt vmcnt(8)" ::: "memory");
            else asm volatile("s_waitcnt vmcnt(0)" ::: "memory");
            __builtin_amdgcn_s_barrier();
            const unsigned char* t0 = lds + (((c & 3) < 2) ? (L_STG + (c & 1) * 32768) : (L_WO + (c & 1) * 32768));
            const unsigned char* t1 = t0 + 16384;
            if (c < 8) {
#pragma unroll
                for (int tt = 0; tt < 2; ++tt)
#pragma unroll
                    for (int u = 0; u < 2; ++u) {
                        v4u hf[2];
#pragma unroll
                        for (int half = 0; half < 2; ++half) {
                            const unsigned off = offB(32 * tt + 16 * half + 4 * G + tq_, 2 * (2 * wave + u) + (tp_ >> 1)) + 8 * (tp_ & 1);
                            v2u q = tr_read(t0 + off), k = tr_read(t1 + off);
                            hf[half] = (v4u){q.x, k.x, q.y, k.y};
                        }
                        bf16x8 lo = as8(hf[0]), hi = as8(hf[1]);
#pragma unroll
                        for (int j = 0; j < 8; ++j) { wqk[u][2 * c + tt][j] = lo[j]; wqk[u][2 * c + tt][8 + j] = hi[j]; }
                    }
            } else if (c < 12) {
#pragma unroll
                for (int j = 0; j < 2; ++j)
#pragma unroll
                    for (int tt = 0; tt < 2; ++tt) {
                        const unsigned char* tb = j ? t1 : t0;
                        v2u lo = tr_read(tb + offB(32 * tt + 8 * G + tq_, 2 * (4 * g + wave) + (tp_ >> 1)) + 8 * (tp_ & 1));
                        v2u hi = tr_read(tb + offB(32 * tt + 8 * G + 4 + tq_, 2 * (4 * g + wave) + (tp_ >> 1)) + 8 * (tp_ & 1));
                        wv[4 * (c - 8) + 2 * j + tt] = mk8(lo, hi);
                    }
            } else {
#pragma unroll
                for (int j = 0; j < 2; ++j)
#pragma unroll
                    for (int kk = 0; kk < 2; ++kk)
#pragma unroll
                        for (int half = 0; half < 2; ++half) {
                            const unsigned char* tb = j ? t1 : t0;
                            const int e0 = 32 * kk + 16 * (G & 1) + 4 * (2 * half + (G >> 1)) + tq_;
                            v2u A = tr_read(tb + offB(e0, 4 * wave + (tp_ >> 1)) + 8 * (tp_ & 1));
                            v2u B = tr_read(tb + offB(e0, 4 * wave + 2 + (tp_ >> 1)) + 8 * (tp_ & 1));
                            const int dp = 8 * (c - 12) + 4 * j + wave;
                            *(v4u*)(lds + L_WO + ((dp * 2 + kk) * 2 + half) * 1024 + lane * 16) = (v4u){A.x, B.x, A.y, B.y};
                        }
            }
            asm volatile("s_waitcnt lgkmcnt(0)" ::: "memory");
            __builtin_amdgcn_s_barrier();
        }
    }
    const int sidx = (lane & 8) ? 0xEEEE : 0x4444;
    const int tq = l15 >> 2, tp = lane & 3;
    const unsigned char* memrd = lds + L_MEM + (8 * G + tq) * 16 + (tp & 1) * 8;
    const unsigned char* trd = lds + L_T + (8 * G + tq) * 32 + (tp & 1) * 8;
    const unsigned char* wp = lds + L_WO + wave * 16384 + lane * 16;
    const float sl2e = 0.08838834764831845f * 1.4426950408889634f;
    const bool exist0 = (G < 2);
    constexpr int PD = 4;
    const __amdgpu_buffer_rsrc_t rk = __builtin_amdgcn_make_buffer_rsrc(a.K, 0, S_ * HD * 2, 0x00020000);
    const __amdgpu_buffer_rsrc_t rv = __builtin_amdgcn_make_buffer_rsrc(a.VT, 0, S_ * HD * 2, 0x00020000);
    const __amdgpu_buffer_rsrc_t rkm = __builtin_amdgcn_make_buffer_rsrc(a.KM, 0, NSEG * 8 * HD * 2, 0x00020000);
    const __amdgpu_buffer_rsrc_t rvm = __builtin_amdgcn_make_buffer_rsrc(a.VMT, 0, NSEG * 8 * HD * 2, 0x00020000);

    wait_flag(a.flags + h * FLAG_STRIDE);
    wait_flag(a.flags + (32 + h) * FLAG_STRIDE);
    const unsigned lds0 = __builtin_amdgcn_readfirstlane(lds_addr(lds));
    const u16* ksrc0; const u16* ksrc1; const u16* vsrc;
    {
        const int p0 = tid, p1 = tid + 256;
        ksrc0 = a.K + (size_t)(p0 >> 4) * HD + h * DH + (((p0 & 15) ^ ((p0 >> 4) & 15)) * 8);
        ksrc1 = a.K + (size_t)(p1 >> 4) * HD + h * DH + (((p1 & 15) ^ ((p1 >> 4) & 15)) * 8);
        vsrc = a.VT + ((size_t)h * DH + 64 * g) * 32 + tid * 8;
    }
    auto load_kv = [&](int s) {
        glds16_sc1(ksrc0 + (size_t)s * 32 * HD, lds0 + L_KX + wave * 1024);
        glds16_sc1(ksrc1 + (size_t)s * 32 * HD, lds0 + L_KX + 4096 + wave * 1024);
        glds16_sc1(vsrc + (size_t)s * NH * DH * 32, lds0 + L_VX + wave * 1024);
    };
    v4u kx[2][4]; v2u vx[2];
    auto load_kx = [&](int s) {
#pragma unroll
        for (int kt = 0; kt < 2; ++kt)
#pragma unroll
            for (int ks = 0; ks < 4; ++ks) kx[kt][ks] = __builtin_amdgcn_raw_buffer_load_b128(rk, (unsigned)((s * 32 + kt * 16 + l15) * HD + h * DH + ks * 32 + 8 * G) * 2, 0, 16);
        const unsigned vo = (unsigned)(((s * NH + h) * DH + 64 * g + wave * 16 + l15) * 32 + 4 * G) * 2;
        vx[0] = __builtin_amdgcn_raw_buffer_load_b64(rv, vo, 0, 16); vx[1] = __builtin_amdgcn_raw_buffer_load_b64(rv, vo + 32, 0, 16);
    };
    load_kx(0);
    unsigned long long tpub = 0;
    DIAG_DECL
    for (int s = 0; s < NSEG; ++s) {
        const int sn = (s + 1 < NSEG) ? s + 1 : s;
        {
            v4u m0, m1;
            unsigned spins = 0;
            for (;;) {
                m0 = __builtin_amdgcn_raw_buffer_load_b128(rmem, s * MEMX_STEP + tid * 16, 0, 16);
                m1 = __builtin_amdgcn_raw_buffer_load_b128(rmem, s * MEMX_STEP + 4096 + tid * 16, 0, 16);
                unsigned mx = umax3(umax3(m0.x, m0.y, m0.z), umax3(m0.w, m1.x, m1.y), max(m1.z, m1.w));
                asm volatile("" ::: "memory");
                if (__all(mx != 0xFFFFFFFFu)) break;
                if (++spins > SPIN_CAP) break;
            }
            *(v4u*)(lds + L_MEM + tid * 16) = m0;
            *(v4u*)(lds + L_MEM + 4096 + tid * 16) = m1;
            asm volatile("s_waitcnt vmcnt(0)" ::: "memory");
        }
        STAMP(0);
        __syncthreads();
        STAMP(1);
        v2u lo[16], hi[16];
        f32x4 acc0 = (f32x4){0.f, 0.f, 0.f, 0.f}, acc1 = acc0;
#pragma unroll
        for (int t = 0; t < PD; ++t) { lo[t] = tr_read(memrd + t * 512); hi[t] = tr_read(memrd + t * 512 + 64); }
        SB0();
#pragma unroll
        for (int t = 0; t < 16; ++t) {
            if (t + PD < 16) { lo[t + PD] = tr_read(memrd + (t + PD) * 512); hi[t + PD] = tr_read(memrd + (t + PD) * 512 + 64); }
            acc0 = __builtin_amdgcn_smfmac_f32_16x16x64_bf16(mk8(lo[t], hi[t]), wqk[0][t], acc0, sidx, 0, 0);
            acc1 = __builtin_amdgcn_smfmac_f32_16x16x64_bf16(mk8(lo[t], hi[t]), wqk[1][t], acc1, sidx, 0, 0);
            SB0();
        }
        {
            v2u v0 = {pack2(acc0[0], acc0[1]), pack2(acc0[2], acc0[3])};
            v2u v1 = {pack2(acc1[0], acc1[1]), pack2(acc1[2], acc1[3])};
            *(v2u*)(lds + L_T + (wave * 32 + l15) * 32 + G * 8) = v0;
            *(v2u*)(lds + L_T + (wave * 32 + 16 + l15) * 32 + G * 8) = v1;
        }
        STAMP(2);
        __syncthreads();
        STAMP(3);
        v2u q_lo[4], q_hi[4], k_lo[4], k_hi[4];
#pragma unroll
        for (int ks = 0; ks < 4; ++ks) {
            q_lo[ks] = tr_read(trd + ks * 1024); q_hi[ks] = tr_read(trd + ks * 1024 + 128);
            k_lo[ks] = tr_read(trd + ks * 1024 + 16); k_hi[ks] = tr_read(trd + ks * 1024 + 128 + 16);
        }
        f32x4 accv = (f32x4){0.f, 0.f, 0.f, 0.f};
#pragma unroll
        for (int t = 0; t < 4; ++t) accv = mfma16(mk8(lo[t], hi[t]), wv[t], accv);
        SB0();
        f32x4 st[3];
#pragma unroll
        for (int i = 0; i < 3; ++i) st[i] = (f32x4){0.f, 0.f, 0.f, 0.f};
#pragma unroll
        for (int ks = 0; ks < 4; ++ks) {
            bf16x8 qf = mk8(q_lo[ks], q_hi[ks]);
            st[1] = mfma16(as8(kx[0][ks]), qf, st[1]);
            st[2] = mfma16(as8(kx[1][ks]), qf, st[2]);
            st[0] = mfma16(mk8(k_lo[ks], k_hi[ks]), qf, st[0]);
        }
        SB0();
#pragma unroll
        for (int t = 4; t < 16; ++t) accv = mfma16(mk8(lo[t], hi[t]), wv[t], accv);
        v4u wo[4][2][2];
#pragma unroll
        for (int i = 0; i < 4; ++i) { wo[i][0][0] = *(const v4u*)(wp + i * 4096); wo[i][0][1] = *(const v4u*)(wp + i * 4096 + 1024); }
        float m = fmaxf(fmaxf(fmaxf(st[1][0], st[1][1]), fmaxf(st[1][2], st[1][3])), fmaxf(fmaxf(st[2][0], st[2][1]), fmaxf(st[2][2], st[2][3])));
        float m0 = fmaxf(fmaxf(st[0][0], st[0][1]), fmaxf(st[0][2], st[0][3]));
        m = exist0 ? fmaxf(m, m0) : m;
        m = xrow16_max(m);
        const float nm = -m * sl2e;
        float v[3][4];
#pragma unroll
        for (int i = 0; i < 3; ++i)
#pragma unroll
            for (int r = 0; r < 4; ++r) v[i][r] = __builtin_amdgcn_exp2f(__builtin_fmaf(st[i][r], sl2e, nm));
#pragma unroll
        for (int r = 0; r < 4; ++r) v[0][r] = exist0 ? v[0][r] : 0.f;
        float sum = ((v[0][0] + v[0][1]) + (v[0][2] + v[0][3])) + ((v[1][0] + v[1][1]) + (v[1][2] + v[1][3])) + ((v[2][0] + v[2][1]) + (v[2][2] + v[2][3]));
        sum = xrow16_sum(sum);
        const float inv = __builtin_amdgcn_rcpf(sum);
        v4u p0 = {pack2(v[0][0] * inv, v[0][1] * inv), pack2(v[0][2] * inv, v[0][3] * inv), pack2(v[1][0] * inv, v[1][1] * inv), pack2(v[1][2] * inv, v[1][3] * inv)};
        v4u p1 = {pack2(v[2][0] * inv, v[2][1] * inv), pack2(v[2][2] * inv, v[2][3] * inv), 0u, 0u};
        v2u vm = {pack2(accv[0], accv[1]), pack2(accv[2], accv[3])};
        f32x4 o = mfma16(mk8(vx[1], (v2u){0u, 0u}), as8(p1), (f32x4){0.f, 0.f, 0.f, 0.f});
        o = mfma16(mk8(vm, vx[0]), as8(p0), o);
        *(v2u*)(lds + L_VM + wave * 512 + lane * 8) = (v2u){pack2(o[0], o[1]), pack2(o[2], o[3])};
        STAMP(4);
        __syncthreads();
        STAMP(5);
        v2u ob[4];
#pragma unroll
        for (int et = 0; et < 4; ++et) ob[et] = *(const v2u*)(lds + L_VM + et * 512 + lane * 8);
#pragma unroll
        for (int i = 0; i < 4; ++i) { wo[i][1][0] = *(const v4u*)(wp + i * 4096 + 2048); wo[i][1][1] = *(const v4u*)(wp + i * 4096 + 3072); }
        SB0();
        const bf16x8 oa0 = mk8(ob[0], ob[1]), oa1 = mk8(ob[2], ob[3]);
        f32x4 d[4];
#pragma unroll
        for (int kk = 0; kk < 2; ++kk)
#pragma unroll
            for (int i = 0; i < 4; ++i) {
                bf16x16 wf;
                bf16x8 w0b = as8(wo[i][kk][0]), w1b = as8(wo[i][kk][1]);
#pragma unroll
                for (int j = 0; j < 8; ++j) { wf[j] = w0b[j]; wf[8 + j] = w1b[j]; }
                d[i] = __builtin_amdgcn_smfmac_f32_16x16x64_bf16(kk == 0 ? oa0 : oa1, wf, kk == 0 ? (f32x4){0.f, 0.f, 0.f, 0.f} : d[i], sidx, 0, 0);
            }
#pragma unroll
        for (int i = 0; i < 4; ++i) {
            const int dcol = 32 * (wave * 4 + i) + 16 * (G >> 1) + l15;
            v2u pv = {nosent(pack2(d[i][0], d[i][1])), nosent(pack2(d[i][2], d[i][3]))};
            __builtin_amdgcn_raw_buffer_store_b64(pv, rpart, (s & 1) * PART_BUF + (dcol >> 3) * 8192 + b * 128 + (dcol & 7) * 16 + (G & 1) * 8, 0, 16);
        }
        STAMP(6);
        if (G < 2) {
            v2u vs = {nosent(vm.x), nosent(vm.y)};
            __builtin_amdgcn_raw_buffer_store_b64(vs, rvm, (unsigned)(((s * NH + h) * DH + 64 * g + wave * 16 + l15) * 8 + 4 * G) * 2, 0, 16);
        }
        if (g == 0 && l15 < 8) {
            v2u kl = wave == 0 ? k_lo[0] : (wave == 1 ? k_lo[1] : (wave == 2 ? k_lo[2] : k_lo[3]));
            v2u kh = wave == 0 ? k_hi[0] : (wave == 1 ? k_hi[1] : (wave == 2 ? k_hi[2] : k_hi[3]));
            v4u kv = {nosent(kl.x), nosent(kl.y), nosent(kh.x), nosent(kh.y)};
            __builtin_amdgcn_raw_buffer_store_b128(kv, rkm, (unsigned)((s * 8 + l15) * HD + h * DH + wave * 32 + 8 * G) * 2, 0, 16);
        }
        SB0();
        load_kx(sn);
        if ((s & 3) == 2 && s + 2 < NSEG) {
            wait_flag(a.flags + (((s + 2) >> 2) * 96 + h) * FLAG_STRIDE);
            wait_flag(a.flags + (((s + 2) >> 2) * 96 + 32 + h) * FLAG_STRIDE);
        }
    }
    DIAG_END;
}

extern "C" void kernel_launch(void* const* d_in, const int* in_sizes, int n_in, void* d_out, int out_size, void* d_ws, size_t ws_size,
                              hipStream_t stream) {
    const u16* x = (const u16*)d_in[0];
    const u16* mem0 = (const u16*)d_in[1];
    const u16* Wq = (const u16*)d_in[2];
    const u16* Wk = (const u16*)d_in[3];
    const u16* Wv = (const u16*)d_in[4];
    const u16* Wo = (const u16*)d_in[5];
    const u16* Wg = (const u16*)d_in[8];
    const u16* Wu = (const u16*)d_in[9];
    const u16* Wd = (const u16*)d_in[10];
    u16* out = (u16*)d_out;

    unsigned char* ws = (unsigned char*)d_ws;
    size_t off = 0;
    auto alloc = [&](size_t bytes) { unsigned char* p = ws + off; off += (bytes + 255) & ~(size_t)255; return p; };
    constexpr size_t KM_BYTES = (size_t)NSEG * 8 * HD * 2;
    constexpr size_t SENT_BYTES = (size_t)2 * PART_BUF + (size_t)(NSEG + 1) * MEMX_STEP + 2 * KM_BYTES;
    unsigned* flags = (unsigned*)alloc((16 * 96 + 64) * FLAG_STRIDE * 4);
    unsigned char* xchg = alloc(SENT_BYTES);
    unsigned char* part = xchg;
    unsigned char* memx = xchg + 2 * PART_BUF;
    u16* KM = (u16*)(memx + (size_t)(NSEG + 1) * MEMX_STEP);
    u16* VMT = (u16*)((unsigned char*)KM + KM_BYTES);
    u16* xn = (u16*)alloc((size_t)S_ * DM * 2);
    u16* Q = (u16*)alloc((size_t)S_ * HD * 2);
    u16* K = (u16*)alloc((size_t)S_ * HD * 2);
    u16* VT = (u16*)alloc((size_t)S_ * HD * 2);
    u16* O = (u16*)alloc((size_t)S_ * HD * 2);
    u16* hb = (u16*)alloc((size_t)S_ * DM * 2);
    u16* hn = (u16*)alloc((size_t)S_ * DM * 2);
    u16* act = (u16*)alloc((size_t)S_ * FF * 2);
    float* Pk = (float*)alloc((size_t)8 * S_ * DM * 4);

    k_prep<<<S_ / 4, 256, 0, stream>>>(x, xn, mem0, (u16*)memx, (v4u*)xchg, (int)(SENT_BYTES / 16), (int)(2 * PART_BUF / 16), (v4u*)flags, (16 * 96 + 64) * FLAG_STRIDE / 4);
    {
        static bool attr_set = false;
        if (!attr_set) { (void)hipFuncSetAttribute((const void*)k_rec, hipFuncAttributeMaxDynamicSharedMemorySize, L_TOTAL); attr_set = true; }
        RecArgs r{Wo, K, VT, Q, O, KM, VMT, memx, part, xn, Wq, Wk, Wv, flags, x, Wg, Wu, Wd, hb, hn, act, out};
        k_rec<<<2 * NX + NWORK, 256, L_TOTAL, stream>>>(r);
    }
    {
        constexpr int ROW0 = RB_IN * 128, MR = S_ - ROW0;
        constexpr int NZ = 8;
        static bool attr2 = false;
        if (!attr2) {
            (void)hipFuncSetAttribute((const void*)k_gemm2<1, 128, 1, 6>, hipFuncAttributeMaxDynamicSharedMemorySize, 6 * (128 * 128 + 8192));
            (void)hipFuncSetAttribute((const void*)k_gemm2<2, 64, 2, 4>, hipFuncAttributeMaxDynamicSharedMemorySize, 4 * (64 * 128 + 16384));
            (void)hipFuncSetAttribute((const void*)k_gemm2<3, 32, 1, 8>, hipFuncAttributeMaxDynamicSharedMemorySize, 8 * (32 * 128 + 8192));
            attr2 = true;
        }
        GemmArgs g1{O, Wo, nullptr, nullptr, nullptr, (u16*)Pk, nullptr, nullptr, HD, DM, HD / NZ, (DM / 64) | (NZ << 16)};
        k_gemm2<1, 128, 1, 6><<<(DM / 64) * (MR / 128) * NZ, 256, 6 * (128 * 128 + 8192), stream>>>(g1, ROW0);
        k_res_ln<NZ><<<MR / 4, 256, 0, stream>>>(Pk, x, hb, hn, ROW0);
        GemmArgs g2{hn, Wg, Wu, nullptr, nullptr, act, nullptr, nullptr, DM, FF, DM, (FF / 64) | (1 << 16)};
        k_gemm2<2, 64, 2, 4><<<(FF / 64) * (MR / 64), 256, 4 * (64 * 128 + 16384), stream>>>(g2, ROW0);
        GemmArgs g3{act, Wd, nullptr, nullptr, hb, out, nullptr, nullptr, FF, DM, FF, (DM / 64) | (1 << 16)};
        k_gemm2<3, 32, 1, 8><<<(DM / 64) * (MR / 32), 256, 8 * (32 * 128 + 8192), stream>>>(g3, ROW0);
    }
}
```

```cpp
#include <hip/hip_runtime.h>
#include <hip/hip_bf16.h>

typedef unsigned short u16;
typedef __bf16 bf16x8 __attribute__((ext_vector_type(8)));
typedef __bf16 bf16x16 __attribute__((ext_vector_type(16)));
typedef float f32x4 __attribute__((ext_vector_type(4)));
typedef unsigned v4u __attribute__((ext_vector_type(4)));
typedef unsigned v2u __attribute__((ext_vector_type(2)));
typedef short v4s __attribute__((ext_vector_type(4)));
#define LAS __attribute__((address_space(3)))

constexpr int S_ = 2048, DM = 512, NH = 32, DH = 128, HD = NH * DH, MEMN = 8, SEG = 32, NSEG = 64, FF = 1792;

__device__ __forceinline__ float bf2f(u16 v) { return __uint_as_float(((unsigned)v) << 16); }
__device__ __forceinline__ u16 f2bf(float f) { return __builtin_bit_cast(u16, (__bf16)f); }
__device__ __forceinline__ float bfr(float f) { return bf2f(f2bf(f)); }
typedef float f32x2 __attribute__((ext_vector_type(2)));
typedef __bf16 bf16x2 __attribute__((ext_vector_type(2)));
__device__ __forceinline__ unsigned pack2(float a, float b) { f32x2 t = {a, b}; return __builtin_bit_cast(unsigned, __builtin_convertvector(t, bf16x2)); }
__device__ __forceinline__ float lo16f(unsigned w) { return __uint_as_float(w << 16); }
__device__ __forceinline__ float hi16f(unsigned w) { return __uint_as_float(w & 0xFFFF0000u); }
template <int CTRL> __device__ __forceinline__ float dppf(float x) {
    return __builtin_bit_cast(float, __builtin_amdgcn_mov_dpp(__builtin_bit_cast(int, x), CTRL, 0xf, 0xf, true));
}
__device__ __forceinline__ float xrow16_max(float x) {
    auto s = __builtin_amdgcn_permlane16_swap(__float_as_uint(x), __float_as_uint(x), false, false);
    x = fmaxf(__uint_as_float(s[0]), __uint_as_float(s[1]));
    auto t = __builtin_amdgcn_permlane32_swap(__float_as_uint(x), __float_as_uint(x), false, false);
    return fmaxf(__uint_as_float(t[0]), __uint_as_float(t[1]));
}
__device__ __forceinline__ float xrow16_sum(float x) {
    auto s = __builtin_amdgcn_permlane16_swap(__float_as_uint(x), __float_as_uint(x), false, false);
    x = __uint_as_float(s[0]) + __uint_as_float(s[1]);
    auto t = __builtin_amdgcn_permlane32_swap(__float_as_uint(x), __float_as_uint(x), false, false);
    return __uint_as_float(t[0]) + __uint_as_float(t[1]);
}
__device__ __forceinline__ unsigned umax3(unsigned a, unsigned b, unsigned c) { return max(max(a, b), c); }
__device__ __forceinline__ unsigned umax4(v4u v) { return max(umax3(v.x, v.y, v.z), v.w); }
__device__ __forceinline__ bool valid16(v4u v) { return umax4(v) != 0xFFFFFFFFu; }
#define SB0() __builtin_amdgcn_sched_barrier(0)
__device__ __forceinline__ void glds16(const void* gsrc, unsigned lds_dst) {
    unsigned keep;
    asm volatile("s_mov_b32 %0, m0\n\ts_mov_b32 m0, %2\n\ts_nop 0\n\tglobal_load_lds_dwordx4 %1, off\n\ts_mov_b32 m0, %0" : "=&s"(keep) : "v"(gsrc), "s"(lds_dst) : "memory");
}
__device__ __forceinline__ void glds16_sc1(const void* gsrc, unsigned lds_dst) {
    unsigned keep;
    asm volatile("s_mov_b32 %0, m0\n\ts_mov_b32 m0, %2\n\ts_nop 0\n\tglobal_load_lds_dwordx4 %1, off sc1\n\ts_mov_b32 m0, %0" : "=&s"(keep) : "v"(gsrc), "s"(lds_dst) : "memory");
}
__device__ __forceinline__ unsigned lds_addr(const void* p) { return (unsigned)(size_t)(LAS const unsigned char*)p; }
__device__ __forceinline__ unsigned nosent(unsigned w) { return min(w, 0xFFFFFFFEu); }
__device__ __forceinline__ v2u tr_read(const void* p) {
    return __builtin_bit_cast(v2u, __builtin_amdgcn_ds_read_tr16_b64_v4i16((LAS v4s*)p));
}
__device__ __forceinline__ bf16x8 mk8(v2u a, v2u b) { v4u t = {a.x, a.y, b.x, b.y}; return __builtin_bit_cast(bf16x8, t); }
__device__ __forceinline__ bf16x8 as8(v4u t) { return __builtin_bit_cast(bf16x8, t); }
__device__ __forceinline__ f32x4 mfma16(bf16x8 a, bf16x8 b, f32x4 c) { return __builtin_amdgcn_mfma_f32_16x16x32_bf16(a, b, c, 0, 0, 0); }

__device__ __forceinline__ float wave_sum(float s) {
    s += dppf<0xB1>(s); s += dppf<0x4E>(s); s += dppf<0x141>(s); s += dppf<0x128>(s);
    return xrow16_sum(s);
}
__device__ __forceinline__ v4u layernorm_vals(v4u raw) {
    float x[8];
#pragma unroll
    for (int i = 0; i < 4; ++i) { x[2 * i] = lo16f(raw[i]); x[2 * i + 1] = hi16f(raw[i]); }
    float s = ((x[0] + x[1]) + (x[2] + x[3])) + ((x[4] + x[5]) + (x[6] + x[7]));
    const float mean = wave_sum(s) * (1.0f / DM);
    float q = 0.f;
#pragma unroll
    for (int i = 0; i < 8; ++i) { float d = x[i] - mean; q += d * d; }
    q = wave_sum(q);
    const float mb = bfr(mean);
    const float vb = bfr(q * (1.0f / DM));
    const float den = bfr(sqrtf(bfr(vb + 1e-5f)));
    v4u outv;
#pragma unroll
    for (int i = 0; i < 4; ++i) {
        float a = bfr(x[2 * i] - mb) / den, b = bfr(x[2 * i + 1] - mb) / den;
        outv[i] = pack2(a, b);
    }
    return outv;
}
__device__ __forceinline__ void layernorm_row(const u16* __restrict__ src, u16* __restrict__ dst, int lane) {
    *(v4u*)(dst + lane * 8) = layernorm_vals(*(const v4u*)(src + lane * 8));
}

__global__ void __launch_bounds__(256) k_prep(const u16* __restrict__ x, u16* __restrict__ xn, const u16* __restrict__ mem0, u16* __restrict__ memx,
                                              v4u* __restrict__ sent, int n_sent16, int memx_off16, v4u* __restrict__ flags, int n_flag16) {
    const int wave = threadIdx.x >> 6, lane = threadIdx.x & 63;
    const int row = blockIdx.x * 4 + wave;
    if (row < S_) layernorm_row(x + (size_t)row * DM, xn + (size_t)row * DM, lane);
    const v4u ones = {0xFFFFFFFFu, 0xFFFFFFFFu, 0xFFFFFFFFu, 0xFFFFFFFFu};
    const int gt = blockIdx.x * 256 + threadIdx.x, nt = gridDim.x * 256;
    for (int i = gt; i < n_sent16; i += nt)
        if (i < memx_off16 || i >= memx_off16 + 512) sent[i] = ones;
    for (int i = gt; i < n_flag16; i += nt) flags[i] = (v4u){0u, 0u, 0u, 0u};
    if (blockIdx.x == 0) {
        for (int i = threadIdx.x; i < MEMN * DM; i += 256) {
            int k = i >> 3, r = i & 7;
            u16 v = mem0[r * DM + k];
            if (v == 0xFFFFu) v = 0x7FFFu;
            memx[i] = v;
        }
    }
}

struct GemmArgs {
    const u16* A; const u16* B0; const u16* B1; const u16* B2; const u16* res;
    u16* C0; u16* C1; u16* C2;
    int lda, ldb, K, pad;
};

__device__ __forceinline__ unsigned offB(unsigned row, unsigned ch) { return 256u * row + 16u * (ch ^ (((row & 3) << 2) | ((row >> 2) & 3))); }
__device__ __forceinline__ unsigned offA(unsigned row, unsigned ch) { return 128u * row + 16u * (ch ^ (row & 7)); }

__device__ __forceinline__ unsigned fB(unsigned row) { return (((row >> 1) & 1) | (((row >> 3) & 1) << 1)) << 1; }
template <int MODE, int BM, int NB, int NST, bool SC1OUT>
__device__ __forceinline__ void gemm_body(const GemmArgs& g, unsigned char* sm, int bm, int bn, int bz) {
    constexpr int MT = BM / 32;
    constexpr int ABYTES = BM * 128, STAGE = ABYTES + NB * 8192;
    constexpr int LA = BM / 32, LPS = LA + 2 * NB;
    const int tid = threadIdx.x, lane = tid & 63;
    const int wave = __builtin_amdgcn_readfirstlane(tid >> 6);
    const int wm = wave >> 1, wn = wave & 1;
    const int kbase = bz * g.K;
    const u16* Asrc[LA];
    const u16* Bsrc[NB][2];
#pragma unroll
    for (int i = 0; i < LA; ++i) { int q = tid + 256 * i; int row = q >> 3, ch = (q & 7) ^ (row & 7); Asrc[i] = g.A + (size_t)(bm + row) * g.lda + kbase + ch * 8; }
#pragma unroll
    for (int n = 0; n < NB; ++n)
#pragma unroll
        for (int i = 0; i < 2; ++i) { int q = tid + 256 * i; int row = q >> 3, ch = (q & 7) ^ fB(row); Bsrc[n][i] = (n == 0 ? g.B0 : g.B1) + (size_t)(kbase + row) * g.ldb + bn + ch * 8; }
    const unsigned sm_base = __builtin_amdgcn_readfirstlane(lds_addr(sm)) + wave * 1024;
    auto stage = [&](int kt) {
        const unsigned base = sm_base + (kt % NST) * STAGE;
#pragma unroll
        for (int i = 0; i < LA; ++i) glds16(Asrc[i] + kt * 64, base + i * 4096);
#pragma unroll
        for (int n = 0; n < NB; ++n)
#pragma unroll
            for (int i = 0; i < 2; ++i) glds16(Bsrc[n][i] + (size_t)kt * 64 * g.ldb, base + ABYTES + n * 8192 + i * 4096);
    };
    f32x4 acc[NB][MT][2];
#pragma unroll
    for (int n = 0; n < NB; ++n)
#pragma unroll
        for (int i = 0; i < MT; ++i) { acc[n][i][0] = (f32x4){0.f, 0.f, 0.f, 0.f}; acc[n][i][1] = acc[n][i][0]; }
    const int nk = g.K / 64;
#pragma unroll
    for (int t = 0; t < NST - 1; ++t) stage(t);
    const int G = lane >> 4, l15 = lane & 15, tq = l15 >> 2, tp = lane & 3;
    for (int kt = 0; kt < nk; ++kt) {
        if (kt + NST - 1 <= nk) asm volatile("s_waitcnt vmcnt(%0)" :: "i"((NST - 2) * LPS) : "memory");
        else asm volatile("s_waitcnt vmcnt(0)" ::: "memory");
        asm volatile("s_waitcnt lgkmcnt(0)" ::: "memory");
        __builtin_amdgcn_s_barrier();
        if (kt + NST - 1 < nk) stage(kt + NST - 1);
        const unsigned char* sA = sm + (kt % NST) * STAGE;
        const unsigned char* sB = sA + ABYTES;
#pragma unroll
        for (int ks = 0; ks < 2; ++ks) {
            bf16x8 af[MT], bq[NB][2];
#pragma unroll
            for (int i = 0; i < MT; ++i) af[i] = as8(*(const v4u*)(sA + offA(wm * (BM / 2) + i * 16 + l15, ks * 4 + G)));
#pragma unroll
            for (int n = 0; n < NB; ++n)
#pragma unroll
                for (int j = 0; j < 2; ++j) {
                    const unsigned r0 = 32 * ks + 8 * G + tq, r1 = r0 + 4, ch = (wn * 2 + j) * 2 + (tp >> 1);
                    v2u lo = tr_read(sB + n * 8192 + r0 * 128 + ((ch ^ fB(r0)) * 16) + 8 * (tp & 1));
                    v2u hi = tr_read(sB + n * 8192 + r1 * 128 + ((ch ^ fB(r1)) * 16) + 8 * (tp & 1));
                    bq[n][j] = mk8(lo, hi);
                }
#pragma unroll
            for (int n = 0; n < NB; ++n)
#pragma unroll
                for (int i = 0; i < MT; ++i)
#pragma unroll
                    for (int j = 0; j < 2; ++j) acc[n][i][j] = mfma16(bq[n][j], af[i], acc[n][i][j]);
        }
    }
#pragma unroll
    for (int i = 0; i < MT; ++i)
#pragma unroll
        for (int j = 0; j < 2; ++j) {
            const int row = bm + wm * (BM / 2) + i * 16 + l15;
            const int col = bn + (wn * 2 + j) * 16 + 4 * G;
            if (MODE == 1) {
                float* P = (float*)g.C0 + ((size_t)bz * S_ + row) * DM + col;
                *(f32x4*)P = acc[0][i][j];
            } else if (MODE == 2) {
                unsigned g0 = pack2(acc[0][i][j][0], acc[0][i][j][1]), g1 = pack2(acc[0][i][j][2], acc[0][i][j][3]);
                unsigned u0 = pack2(acc[NB - 1][i][j][0], acc[NB - 1][i][j][1]), u1 = pack2(acc[NB - 1][i][j][2], acc[NB - 1][i][j][3]);
                v2u v = {pack2(lo16f(g0) * lo16f(u0), hi16f(g0) * hi16f(u0)), pack2(lo16f(g1) * lo16f(u1), hi16f(g1) * hi16f(u1))};
                if (SC1OUT) __hip_atomic_store((unsigned long long*)(g.C0 + (size_t)row * FF + col), __builtin_bit_cast(unsigned long long, v), __ATOMIC_RELAXED, __HIP_MEMORY_SCOPE_AGENT);
                else *(v2u*)(g.C0 + (size_t)row * FF + col) = v;
            } else {
                v2u r = *(const v2u*)(g.res + (size_t)row * DM + col);
                unsigned a0 = pack2(acc[0][i][j][0], acc[0][i][j][1]), a1 = pack2(acc[0][i][j][2], acc[0][i][j][3]);
                v2u v = {pack2(lo16f(r.x) + lo16f(a0), hi16f(r.x) + hi16f(a0)), pack2(lo16f(r.y) + lo16f(a1), hi16f(r.y) + hi16f(a1))};
                if (SC1OUT) __hip_atomic_store((unsigned long long*)(g.C0 + (size_t)row * DM + col), __builtin_bit_cast(unsigned long long, v), __ATOMIC_RELAXED, __HIP_MEMORY_SCOPE_AGENT);
                else *(v2u*)(g.C0 + (size_t)row * DM + col) = v;
            }
        }
}

template <int MODE, int BM, int NB, int NST>
__global__ void __launch_bounds__(256) k_gemm2(GemmArgs g, int row0) {
    extern __shared__ __attribute__((aligned(16))) unsigned char sm[];
    const int ncol = g.pad & 0xFFFF, nz = g.pad >> 16;
    const int per_xcd = gridDim.x >> 3;
    const int lin = (blockIdx.x & 7) * per_xcd + (blockIdx.x >> 3);
    const int panel = lin / ncol;
    gemm_body<MODE, BM, NB, NST, false>(g, sm, row0 + (panel / nz) * BM, (lin % ncol) * 64, panel % nz);
}

template <int NZ>
__global__ void __launch_bounds__(256) k_res_ln(const float* __restrict__ P, const u16* __restrict__ x, u16* __restrict__ hb, u16* __restrict__ hn, int row0) {
    const int wave = threadIdx.x >> 6, lane = threadIdx.x & 63;
    const int row = row0 + blockIdx.x * 4 + wave;
    f32x4 s0 = *(const f32x4*)(P + (size_t)row * DM + lane * 8), s1 = *(const f32x4*)(P + (size_t)row * DM + lane * 8 + 4);
#pragma unroll
    for (int z = 1; z < NZ; ++z) { s0 += *(const f32x4*)(P + ((size_t)z * S_ + row) * DM + lane * 8); s1 += *(const f32x4*)(P + ((size_t)z * S_ + row) * DM + lane * 8 + 4); }
    v4u raw = *(const v4u*)(x + (size_t)row * DM + lane * 8);
    unsigned a0 = pack2(s0[0], s0[1]), a1 = pack2(s0[2], s0[3]), a2 = pack2(s1[0], s1[1]), a3 = pack2(s1[2], s1[3]);
    v4u hv = {pack2(lo16f(raw.x) + lo16f(a0), hi16f(raw.x) + hi16f(a0)), pack2(lo16f(raw.y) + lo16f(a1), hi16f(raw.y) + hi16f(a1)),
              pack2(lo16f(raw.z) + lo16f(a2), hi16f(raw.z) + hi16f(a2)), pack2(lo16f(raw.w) + lo16f(a3), hi16f(raw.w) + hi16f(a3))};
    *(v4u*)(hb + (size_t)row * DM + lane * 8) = hv;
    *(v4u*)(hn + (size_t)row * DM + lane * 8) = layernorm_vals(hv);
}

#define KX_DMA 0
#define XDELAY 0
#define YDELAY 0
#define YSIG 1
#define SIGPOS 4
#define WOEARLY 0
#define PINA 0
#define YMULTI 0
#define YSTAG 12
#define YADAPT 0
#define YINIT 0
#define YDN 4
#define YUP 12
constexpr int NX = 64;
constexpr int MEMX_STEP = 8192;
constexpr int PART_BUF = 64 * 64 * 128;
constexpr unsigned SPIN_CAP = 1u << 22;

#define WSTAMP() do {} while (0)
#define STAMP(i) do {} while (0)
#define DIAG_DECL
#define DIAG_END
struct RecArgs {
    const u16* Wo;
    u16* K; u16* VT; u16* Q; u16* O;
    u16* KM; u16* VMT;
    unsigned char* memx; unsigned char* part;
    const u16* xn; const u16* Wq; const u16* Wk; const u16* Wv;
    unsigned* flags;
    const u16* x; const u16* Wg; const u16* Wu; const u16* Wd;
    u16* hb; u16* hn; u16* act; u16* out;
};
#define RBIN 12
constexpr int RB_IN = RBIN;
constexpr int SIG_BASE = 16 * 96 + 64;
constexpr int CNT_BASE = 16 * 96;
constexpr int NWORK = 128;
constexpr int FLAG_STRIDE = 16;

__device__ __forceinline__ void wait_flag(const unsigned* f) {
    unsigned spins = 0;
    while (__builtin_amdgcn_readfirstlane(__hip_atomic_load(f, __ATOMIC_RELAXED, __HIP_MEMORY_SCOPE_AGENT)) == 0u) {
        __builtin_amdgcn_s_sleep(2);
        if (++spins > SPIN_CAP) break;
    }
    asm volatile("" ::: "memory");
}

__device__ __forceinline__ void wait_count_acquire(const unsigned* c, unsigned target) {
    if (threadIdx.x < 64) {
        unsigned spins = 0;
        while (__builtin_amdgcn_readfirstlane(__hip_atomic_load(c, __ATOMIC_RELAXED, __HIP_MEMORY_SCOPE_AGENT)) < target) {
            __builtin_amdgcn_s_sleep(32);
            if (++spins > SPIN_CAP) break;
        }
        __builtin_amdgcn_fence(__ATOMIC_ACQUIRE, "agent");
        asm volatile("s_waitcnt vmcnt(0)" ::: "memory");
    }
    __syncthreads();
}
__device__ __forceinline__ void signal_count(unsigned* c) {
    asm volatile("s_waitcnt vmcnt(0)" ::: "memory");
    __syncthreads();
    if (threadIdx.x == 0) __hip_atomic_fetch_add(c, 1u, __ATOMIC_RELAXED, __HIP_MEMORY_SCOPE_AGENT);
}

template <bool ISV>
__device__ __forceinline__ void proj_col(const RecArgs& a, unsigned char* lds, int c) {
    constexpr int NST = 8;
    const int tid = threadIdx.x, lane = tid & 63;
    const int wave = __builtin_amdgcn_readfirstlane(tid >> 6);
    const int wm = wave >> 1, wn = wave & 1;
    const int mat = c >> 5, head = c & 31;
    const int G = lane >> 4, l15 = lane & 15, tq = l15 >> 2, tp = lane & 3;
    const unsigned sbase = __builtin_amdgcn_readfirstlane(lds_addr(lds)) + wave * 1024;
    bf16x8 bw[4][16];
    {
        const u16* Bp = (mat == 0 ? a.Wk : (mat == 1 ? a.Wv : a.Wq)) + head * DH;
#pragma unroll
        for (int kt = 0; kt < 8; ++kt)
#pragma unroll
            for (int i = 0; i < 4; ++i) {
                const int q = tid + 256 * i, r = q >> 4;
                glds16(Bp + (size_t)(kt * 64 + r) * HD + (((q & 15) ^ (((r & 3) << 2) | ((r >> 2) & 3))) * 8), sbase + kt * 16384 + i * 4096);
            }
        asm volatile("s_waitcnt vmcnt(0)" ::: "memory");
        __builtin_amdgcn_s_barrier();
#pragma unroll
        for (int t = 0; t < 16; ++t)
#pragma unroll
            for (int j = 0; j < 4; ++j) {
                const unsigned char* sB = lds + (t >> 1) * 16384;
                const int cc = wn * 4 + j, ks = t & 1;
                v2u lo = tr_read(sB + offB(32 * ks + 8 * G + tq, 2 * cc + (tp >> 1)) + 8 * (tp & 1));
                v2u hi = tr_read(sB + offB(32 * ks + 8 * G + 4 + tq, 2 * cc + (tp >> 1)) + 8 * (tp & 1));
                bw[j][t] = mk8(lo, hi);
            }
        asm volatile("s_waitcnt lgkmcnt(0)" ::: "memory");
        __builtin_amdgcn_s_barrier();
    }
    const u16* asrc[4];
#pragma unroll
    for (int i = 0; i < 4; ++i) { const int q = tid + 256 * i; asrc[i] = a.xn + (size_t)(q >> 3) * DM + (((q & 7) ^ ((q >> 3) & 7)) * 8); }
    auto stage = [&](int n) {
        const unsigned d = sbase + (n & (NST - 1)) * 16384;
        const size_t off = (size_t)(n >> 3) * 128 * DM + (n & 7) * 64;
#pragma unroll
        for (int i = 0; i < 4; ++i) glds16(asrc[i] + off, d + i * 4096);
    };
#pragma unroll
    for (int n = 0; n < NST - 1; ++n) stage(n);
    unsigned char* img = lds + 131072;
#pragma unroll 1
    for (int rb = 0; rb < 16; ++rb) {
        f32x4 acc[4][4];
#pragma unroll
        for (int i = 0; i < 4; ++i)
#pragma unroll
            for (int j = 0; j < 4; ++j) acc[i][j] = (f32x4){0.f, 0.f, 0.f, 0.f};
#pragma unroll
        for (int kt = 0; kt < 8; ++kt) {
            const int n = rb * 8 + kt;
            if (n + NST - 1 > 128) asm volatile("s_waitcnt vmcnt(0)" ::: "memory");
            else if (kt < 7 && rb > 0) asm volatile("s_waitcnt vmcnt(32)" ::: "memory");
            else asm volatile("s_waitcnt vmcnt(24)" ::: "memory");
            asm volatile("s_waitcnt lgkmcnt(0)" ::: "memory");
            __builtin_amdgcn_s_barrier();
            if (kt == 7 && rb >= 2 && tid == 0) __hip_atomic_store(a.flags + ((rb - 1) * 96 + c) * FLAG_STRIDE, 1u, __ATOMIC_RELAXED, __HIP_MEMORY_SCOPE_AGENT);
            if (n + NST - 1 < 128) stage(n + NST - 1);
            const unsigned char* sA = lds + kt * 16384;
#pragma unroll
            for (int ks = 0; ks < 2; ++ks) {
                bf16x8 af[4];
#pragma unroll
                for (int i = 0; i < 4; ++i) af[i] = as8(*(const v4u*)(sA + offA(wm * 64 + i * 16 + l15, ks * 4 + G)));
#pragma unroll
                for (int i = 0; i < 4; ++i)
#pragma unroll
                    for (int j = 0; j < 4; ++j) acc[i][j] = ISV ? mfma16(af[i], bw[j][kt * 2 + ks], acc[i][j]) : mfma16(bw[j][kt * 2 + ks], af[i], acc[i][j]);
            }
        }
#pragma unroll
        for (int i = 0; i < 4; ++i)
#pragma unroll
            for (int j = 0; j < 4; ++j) {
                v2u v = {pack2(acc[i][j][0], acc[i][j][1]), pack2(acc[i][j][2], acc[i][j][3])};
                if (ISV) {
                    const int e = (wn * 4 + j) * 16 + l15, row0 = wm * 64 + i * 16 + 4 * G;
                    const int line = (row0 >> 5) * 128 + e;
                    *(v2u*)(img + line * 64 + ((((row0 & 31) >> 2) ^ (e & 7)) * 8)) = v;
                } else {
                    const int row = wm * 64 + i * 16 + l15, col0 = (wn * 4 + j) * 16 + 4 * G;
                    *(v2u*)(img + row * 256 + (((col0 >> 3) ^ (row & 15)) * 16) + ((col0 & 4) * 2)) = v;
                }
            }
        asm volatile("s_waitcnt lgkmcnt(0)" ::: "memory");
        __builtin_amdgcn_s_barrier();
        if (ISV) {
            const __amdgpu_buffer_rsrc_t r = __builtin_amdgcn_make_buffer_rsrc(a.VT, 0, S_ * HD * 2, 0x00020000);
#pragma unroll
            for (int i = 0; i < 8; ++i) {
                const int q = tid + 256 * i;
                const int line = q >> 2, cq = q & 3, e = line & 127;
                v2u x0 = *(const v2u*)(img + line * 64 + (((2 * cq) ^ (e & 7)) * 8));
                v2u x1 = *(const v2u*)(img + line * 64 + (((2 * cq + 1) ^ (e & 7)) * 8));
                v4u v = {x0.x, x0.y, x1.x, x1.y};
                const int seg = rb * 4 + (line >> 7);
                __builtin_amdgcn_raw_buffer_store_b128(v, r, (unsigned)((((seg * NH + head) * DH + e) * 32 + cq * 8) * 2), 0, 16);
            }
        } else {
            const __amdgpu_buffer_rsrc_t r = __builtin_amdgcn_make_buffer_rsrc(mat == 0 ? a.K : a.Q, 0, S_ * HD * 2, 0x00020000);
#pragma unroll
            for (int i = 0; i < 8; ++i) {
                const int q = tid + 256 * i;
                const int row = q >> 4, ch = q & 15;
                v4u v = *(const v4u*)(img + row * 256 + ((ch ^ (row & 15)) * 16));
                __builtin_amdgcn_raw_buffer_store_b128(v, r, (unsigned)(((rb * 128 + row) * HD + head * DH + ch * 8) * 2), 0, 16);
            }
        }
        if (rb == 0 || rb == 15) {
            asm volatile("s_waitcnt vmcnt(0) lgkmcnt(0)" ::: "memory");
            __builtin_amdgcn_s_barrier();
            if (tid == 0) __hip_atomic_store(a.flags + (rb * 96 + c) * FLAG_STRIDE, 1u, __ATOMIC_RELAXED, __HIP_MEMORY_SCOPE_AGENT);
        }
    }
    asm volatile("s_waitcnt vmcnt(0)" ::: "memory");
    __syncthreads();
}

__device__ __forceinline__ void attn_unit(const RecArgs& a, int seg, int h, int lane) {
    const int G = lane >> 4, l15 = lane & 15;
    const float scale = 0.08838834764831845f;
    const __amdgpu_buffer_rsrc_t rq = __builtin_amdgcn_make_buffer_rsrc(a.Q, 0, S_ * HD * 2, 0x00020000);
    const __amdgpu_buffer_rsrc_t rk = __builtin_amdgcn_make_buffer_rsrc(a.K, 0, S_ * HD * 2, 0x00020000);
    const __amdgpu_buffer_rsrc_t rv = __builtin_amdgcn_make_buffer_rsrc(a.VT, 0, S_ * HD * 2, 0x00020000);
    const __amdgpu_buffer_rsrc_t rkm = __builtin_amdgcn_make_buffer_rsrc(a.KM, 0, NSEG * 8 * HD * 2, 0x00020000);
    const __amdgpu_buffer_rsrc_t rvm = __builtin_amdgcn_make_buffer_rsrc(a.VMT, 0, NSEG * 8 * HD * 2, 0x00020000);
    const int rb = seg >> 2;
    wait_flag(a.flags + (rb * 96 + h) * FLAG_STRIDE);
    wait_flag(a.flags + (rb * 96 + 32 + h) * FLAG_STRIDE);
    wait_flag(a.flags + (rb * 96 + 64 + h) * FLAG_STRIDE);
    v4u k1[4], k2[4], q0[4], q1[4];
    v2u v1[8], v2[8];
#pragma unroll
    for (int ks = 0; ks < 4; ++ks) {
        const unsigned eo = (unsigned)(h * DH + ks * 32 + 8 * G) * 2;
        k1[ks] = __builtin_amdgcn_raw_buffer_load_b128(rk, (unsigned)(seg * 32 + l15) * (HD * 2) + eo, 0, 16);
        k2[ks] = __builtin_amdgcn_raw_buffer_load_b128(rk, (unsigned)(seg * 32 + 16 + l15) * (HD * 2) + eo, 0, 16);
        q0[ks] = __builtin_amdgcn_raw_buffer_load_b128(rq, (unsigned)(seg * 32 + l15) * (HD * 2) + eo, 0, 16);
        q1[ks] = __builtin_amdgcn_raw_buffer_load_b128(rq, (unsigned)(seg * 32 + 16 + l15) * (HD * 2) + eo, 0, 16);
    }
#pragma unroll
    for (int et = 0; et < 8; ++et) {
        const unsigned vo = (unsigned)(((seg * NH + h) * DH + et * 16 + l15) * 32 + 4 * G) * 2;
        v1[et] = __builtin_amdgcn_raw_buffer_load_b64(rv, vo, 0, 16);
        v2[et] = __builtin_amdgcn_raw_buffer_load_b64(rv, vo + 32, 0, 16);
    }
    v4u k0[4]; v2u vm[8];
    {
        unsigned spins = 0;
        const unsigned* probe = (const unsigned*)(a.VMT + ((size_t)(seg * NH + h) * DH + 127) * 8);
        while (__builtin_amdgcn_readfirstlane(__hip_atomic_load(probe, __ATOMIC_RELAXED, __HIP_MEMORY_SCOPE_AGENT)) == 0xFFFFFFFFu) {
            if (seg >= NSEG - 2) __builtin_amdgcn_s_sleep(10); else __builtin_amdgcn_s_sleep(100);
            if (++spins > SPIN_CAP) break;
        }
        for (;;) {
            unsigned mx = 0;
#pragma unroll
            for (int ks = 0; ks < 4; ++ks) {
                k0[ks] = __builtin_amdgcn_raw_buffer_load_b128(rkm, (unsigned)((seg * 8 + (l15 & 7)) * HD + h * DH + ks * 32 + 8 * G) * 2, 0, 16);
            }
#pragma unroll
            for (int et = 0; et < 8; ++et) vm[et] = __builtin_amdgcn_raw_buffer_load_b64(rvm, (unsigned)(((seg * NH + h) * DH + et * 16 + l15) * 8 + 4 * (G & 1)) * 2, 0, 16);
#pragma unroll
            for (int ks = 0; ks < 4; ++ks) mx = max(mx, umax4(k0[ks]));
#pragma unroll
            for (int et = 0; et < 8; ++et) mx = umax3(mx, vm[et].x, vm[et].y);
            asm volatile("" ::: "memory");
            if (__all(mx != 0xFFFFFFFFu)) break;
            __builtin_amdgcn_s_sleep(16);
            if (++spins > SPIN_CAP) break;
        }
    }
    f32x4 st[3][2];
#pragma unroll
    for (int i = 0; i < 3; ++i) { st[i][0] = (f32x4){0.f, 0.f, 0.f, 0.f}; st[i][1] = st[i][0]; }
#pragma unroll
    for (int ks = 0; ks < 4; ++ks) {
        st[0][0] = mfma16(as8(k0[ks]), as8(q0[ks]), st[0][0]); st[0][1] = mfma16(as8(k0[ks]), as8(q1[ks]), st[0][1]);
        st[1][0] = mfma16(as8(k1[ks]), as8(q0[ks]), st[1][0]); st[1][1] = mfma16(as8(k1[ks]), as8(q1[ks]), st[1][1]);
        st[2][0] = mfma16(as8(k2[ks]), as8(q0[ks]), st[2][0]); st[2][1] = mfma16(as8(k2[ks]), as8(q1[ks]), st[2][1]);
    }
    bf16x8 pf[2][2];
#pragma unroll
    for (int qt = 0; qt < 2; ++qt) {
        const int t = qt * 16 + l15;
        float v[3][4];
        float m = -3.0e38f;
#pragma unroll
        for (int i = 0; i < 3; ++i)
#pragma unroll
            for (int r = 0; r < 4; ++r) {
                float sc = bfr(bfr(st[i][qt][r]) * scale);
                bool ok;
                if (i == 0) ok = (G < 2); else ok = ((i - 1) * 16 + 4 * G + r) <= t;
                bool exist = (i != 0) || (G < 2);
                sc = ok ? sc : bfr(-1.0e9f);
                v[i][r] = exist ? sc : -3.0e38f;
                m = fmaxf(m, v[i][r]);
            }
        m = xrow16_max(m);
        float sum = 0.f;
#pragma unroll
        for (int i = 0; i < 3; ++i)
#pragma unroll
            for (int r = 0; r < 4; ++r) { bool exist = (i != 0) || (G < 2); v[i][r] = exist ? __expf(v[i][r] - m) : 0.f; sum += v[i][r]; }
        sum = xrow16_sum(sum);
        const float inv = 1.0f / sum;
        v4u p0 = {pack2(v[0][0] * inv, v[0][1] * inv), pack2(v[0][2] * inv, v[0][3] * inv), pack2(v[1][0] * inv, v[1][1] * inv), pack2(v[1][2] * inv, v[1][3] * inv)};
        v4u p1 = {pack2(v[2][0] * inv, v[2][1] * inv), pack2(v[2][2] * inv, v[2][3] * inv), 0u, 0u};
        pf[qt][0] = as8(p0); pf[qt][1] = as8(p1);
    }
#pragma unroll
    for (int et = 0; et < 8; ++et) {
        bf16x8 a0 = mk8(vm[et], v1[et]);
        bf16x8 a1 = mk8(v2[et], (v2u){0u, 0u});
#pragma unroll
        for (int qt = 0; qt < 2; ++qt) {
            f32x4 o = (f32x4){0.f, 0.f, 0.f, 0.f};
            o = mfma16(a0, pf[qt][0], o);
            o = mfma16(a1, pf[qt][1], o);
            v2u ov = {pack2(o[0], o[1]), pack2(o[2], o[3])};
            __hip_atomic_store((unsigned long long*)(a.O + ((size_t)seg * 32 + qt * 16 + l15) * HD + h * DH + et * 16 + 4 * G), __builtin_bit_cast(unsigned long long, ov), __ATOMIC_RELAXED, __HIP_MEMORY_SCOPE_AGENT);
        }
    }
}

constexpr int L_WO = 0;
constexpr int L_MEM = 65536;
constexpr int L_T = L_MEM + 8192;
constexpr int L_VM = L_T + 4096;
constexpr int L_KX = L_VM + 2048;
constexpr int L_VX = L_KX + 8192;
constexpr int L_STG = 96 * 1024;
constexpr int L_TOTAL = 160 * 1024;

#define XSIG(pos) do { if (SIGPOS == (pos) && tid == 0) __hip_atomic_store(a.flags + (SIG_BASE + 2 * b) * FLAG_STRIDE, (unsigned)(s + 1), __ATOMIC_RELAXED, __HIP_MEMORY_SCOPE_AGENT); } while (0)
__global__ void __launch_bounds__(256, 1) k_rec(RecArgs a) {
    extern __shared__ __attribute__((aligned(16))) unsigned char lds[];
    const int tid = threadIdx.x, lane = tid & 63;
    const int wave = __builtin_amdgcn_readfirstlane(tid >> 6);
    const int G = lane >> 4, l15 = lane & 15;
    const __amdgpu_buffer_rsrc_t rmem = __builtin_amdgcn_make_buffer_rsrc(a.memx, 0, (NSEG + 1) * MEMX_STEP, 0x00020000);
    const __amdgpu_buffer_rsrc_t rpart = __builtin_amdgcn_make_buffer_rsrc(a.part, 0, 2 * PART_BUF, 0x00020000);

#define XCDMAP 0
    const int bid = blockIdx.x;
    if (bid >= 2 * NX) {
        const int w = bid - 2 * NX;
        int wsn_ = 0; (void)wsn_;
        WSTAMP();
        if (w >= 32) {
            const int c = w - 32;
            if ((c >> 5) == 1) proj_col<true>(a, lds, c); else proj_col<false>(a, lds, c);
        }
        WSTAMP();
        if (w < 32) {
            for (int rb = 0; rb < 16; ++rb) {
                attn_unit(a, rb * 4 + (w >> 3), (w & 7) * 4 + wave, lane);
                signal_count(a.flags + (CNT_BASE + rb * 4) * FLAG_STRIDE);
                WSTAMP();
            }
        } else if (w < 80) {
            const int t = (w - 32) & 15;
            for (int rb = (w - 32) >> 4; rb < RB_IN; rb += 3) {
                unsigned* cnt = a.flags + (CNT_BASE + rb * 4) * FLAG_STRIDE;
                wait_count_acquire(cnt, 32);
                WSTAMP();
                GemmArgs g{a.O, a.Wo, nullptr, nullptr, a.x, a.hb, nullptr, nullptr, HD, DM, HD, 0};
                gemm_body<3, 64, 1, 8, true>(g, lds, rb * 128 + (t >> 3) * 64, (t & 7) * 64, 0);
                signal_count(cnt + FLAG_STRIDE);
                WSTAMP();
                wait_count_acquire(cnt + FLAG_STRIDE, 16);
                const __amdgpu_buffer_rsrc_t rh = __builtin_amdgcn_make_buffer_rsrc(a.hn, 0, S_ * DM * 2, 0x00020000);
                const int row0 = rb * 128 + t * 8 + wave * 2;
                v4u h0 = *(const v4u*)(a.hb + (size_t)row0 * DM + lane * 8), h1 = *(const v4u*)(a.hb + (size_t)(row0 + 1) * DM + lane * 8);
                __builtin_amdgcn_raw_buffer_store_b128(layernorm_vals(h0), rh, (unsigned)(row0 * DM + lane * 8) * 2, 0, 16);
                __builtin_amdgcn_raw_buffer_store_b128(layernorm_vals(h1), rh, (unsigned)((row0 + 1) * DM + lane * 8) * 2, 0, 16);
                signal_count(cnt + 2 * FLAG_STRIDE);
            }
        } else if (w < 112) {
            for (int u = w - 80; u < RB_IN * 28; u += 32) {
                const int rb = u / 28;
                unsigned* cnt = a.flags + (CNT_BASE + rb * 4) * FLAG_STRIDE;
                wait_count_acquire(cnt + 2 * FLAG_STRIDE, 16);
                WSTAMP();
                GemmArgs g{a.hn, a.Wg, a.Wu, nullptr, nullptr, a.act, nullptr, nullptr, DM, FF, DM, 0};
                gemm_body<2, 128, 2, 4, true>(g, lds, rb * 128, (u % 28) * 64, 0);
                signal_count(cnt + 3 * FLAG_STRIDE);
                WSTAMP();
            }
        } else {
            for (int rb = (w - 112) >> 3; rb < RB_IN; rb += 2) {
                unsigned* cnt = a.flags + (CNT_BASE + rb * 4) * FLAG_STRIDE;
                wait_count_acquire(cnt + 3 * FLAG_STRIDE, 28);
                WSTAMP();
                GemmArgs g{a.act, a.Wd, nullptr, nullptr, a.hb, a.out, nullptr, nullptr, FF, DM, FF, 0};
                gemm_body<3, 128, 1, 6, false>(g, lds, rb * 128, (w & 7) * 64, 0);
                __syncthreads();
                WSTAMP();
            }
        }
        return;
    }
    if (bid >= NX) {
        if (wave != 0) return;
        const int y = bid - NX;
        DIAG_DECL
        unsigned long long typub = 0;
        unsigned ydel = YINIT, ydmin = 1000u; (void)ydel; (void)ydmin;
        bf16x8 sel;
#pragma unroll
        for (int j = 0; j < 8; ++j) sel[j] = (l15 == j) ? (__bf16)1.0f : (__bf16)0.0f;
        for (int s = 0; s < NSEG - 1; ++s) {
            const unsigned base = (s & 1) * PART_BUF + y * 8192 + lane * 16;
            v4u m[8];
            unsigned spins = 0;
            {
                unsigned sp2 = 0;
                while (__builtin_amdgcn_readfirstlane(__hip_atomic_load(a.flags + (SIG_BASE + 2 * y) * FLAG_STRIDE, __ATOMIC_RELAXED, __HIP_MEMORY_SCOPE_AGENT)) < (unsigned)(s + 1)) {
                    if (++sp2 > SPIN_CAP) break;
                }
                asm volatile("" ::: "memory");
            }
            f32x4 acc0, acc1;
            for (;;) {
#pragma unroll
                for (int t = 0; t < 8; ++t) m[t] = __builtin_amdgcn_raw_buffer_load_b128(rpart, base + t * 1024, 0, 16);
                unsigned mx = 0;
                acc0 = (f32x4){0.f, 0.f, 0.f, 0.f}; acc1 = acc0;
#pragma unroll
                for (int t = 0; t < 8; t += 2) {
                    acc0 = mfma16(sel, as8(m[t]), acc0); acc1 = mfma16(sel, as8(m[t + 1]), acc1);
                    mx = umax3(mx, umax3(m[t].x, m[t].y, m[t].z), m[t].w);
                    mx = umax3(mx, umax3(m[t + 1].x, m[t + 1].y, m[t + 1].z), m[t + 1].w);
                }
                asm volatile("" : "+v"(acc0), "+v"(acc1) :: "memory");
                if (__all(mx != 0xFFFFFFFFu)) break;
                if (++spins > SPIN_CAP) break;
            }
            if (lane == 0) __hip_atomic_store(a.flags + (SIG_BASE + 128 + 2 * y) * FLAG_STRIDE, (unsigned)(s + 1), __ATOMIC_RELAXED, __HIP_MEMORY_SCOPE_AGENT);
            STAMP(0);
            acc0 += acc1;
#pragma unroll
            for (int r = 0; r < 4; ++r) acc0[r] += dppf<0x128>(acc0[r]);
            if (lane < 32 && l15 < 8) {
                v2u v = {nosent(pack2(acc0[0], acc0[1])), nosent(pack2(acc0[2], acc0[3]))};
                __builtin_amdgcn_raw_buffer_store_b64(v, rmem, (s + 1) * MEMX_STEP + y * 128 + l15 * 16 + G * 8, 0, 16);
            }
            STAMP(1);
            const v4u sent = {0xFFFFFFFFu, 0xFFFFFFFFu, 0xFFFFFFFFu, 0xFFFFFFFFu};
#pragma unroll
            for (int t = 0; t < 8; ++t) __builtin_amdgcn_raw_buffer_store_b128(sent, rpart, base + t * 1024, 0, 16);
            asm volatile("s_waitcnt vmcnt(0)" ::: "memory");
        }
        DIAG_END;
        return;
    }

    const int b = bid, h = b >> 1, g = b & 1;
    bf16x16 wqk[2][16];
    bf16x8 wv[16];
    {
        const int tq_ = l15 >> 2, tp_ = lane & 3;
        const unsigned stg = __builtin_amdgcn_readfirstlane(lds_addr(lds)) + wave * 1024;
        int srow[4], sch[4];
#pragma unroll
        for (int i = 0; i < 4; ++i) { const int q = tid + 256 * i; srow[i] = q >> 4; sch[i] = ((q & 15) ^ (((srow[i] & 3) << 2) | ((srow[i] >> 2) & 3))) * 8; }
        auto issue = [&](int c) {
            const u16* s0; const u16* s1; int ld;
            if (c < 8) { s0 = a.Wq + (size_t)(64 * c) * HD + h * DH; s1 = a.Wk + (size_t)(64 * c) * HD + h * DH; ld = HD; }
            else if (c < 12) { s0 = a.Wv + (size_t)(128 * (c - 8)) * HD + h * DH; s1 = s0 + (size_t)64 * HD; ld = HD; }
            else { s0 = a.Wo + (size_t)(h * DH + 64 * g) * DM + 256 * (c - 12); s1 = s0 + 128; ld = DM; }
            const unsigned dst = stg + (((c & 3) < 2) ? (unsigned)(L_STG + (c & 1) * 32768) : (unsigned)(L_WO + (c & 1) * 32768));
#pragma unroll
            for (int i = 0; i < 4; ++i) glds16(s0 + (size_t)srow[i] * ld + sch[i], dst + i * 4096);
#pragma unroll
            for (int i = 0; i < 4; ++i) glds16(s1 + (size_t)srow[i] * ld + sch[i], dst + 16384 + i * 4096);
        };
        issue(0); issue(1); issue(2);
#pragma unroll
        for (int c = 0; c < 14; ++c) {
            if (c + 3 < 14) { issue(c + 3); asm volatile("s_waitcnt vmcnt(24)" ::: "memory"); }
            else if (c + 3 == 14) asm volatile("s_waitcnt vmcnt(16)" ::: "memory");
            else if (c + 2 == 14) asm volatile("s_waitcnt vmcnt(8)" ::: "memory");
            else asm volatile("s_waitcnt vmcnt(0)" ::: "memory");
            __builtin_amdgcn_s_barrier();
            const unsigned char* t0 = lds + (((c & 3) < 2) ? (L_STG + (c & 1) * 32768) : (L_WO + (c & 1) * 32768));
            const unsigned char* t1 = t0 + 16384;
            if (c < 8) {
#pragma unroll
                for (int tt = 0; tt < 2; ++tt)
#pragma unroll
                    for (int u = 0; u < 2; ++u) {
                        v4u hf[2];
#pragma unroll
                        for (int half = 0; half < 2; ++half) {
                            const unsigned off = offB(32 * tt + 16 * half + 4 * G + tq_, 2 * (2 * wave + u) + (tp_ >> 1)) + 8 * (tp_ & 1);
                            v2u q = tr_read(t0 + off), k = tr_read(t1 + off);
                            hf[half] = (v4u){q.x, k.x, q.y, k.y};
                        }
                        bf16x8 lo = as8(hf[0]), hi = as8(hf[1]);
#pragma unroll
                        for (int j = 0; j < 8; ++j) { wqk[u][2 * c + tt][j] = lo[j]; wqk[u][2 * c + tt][8 + j] = hi[j]; }
                    }
            } else if (c < 12) {
#pragma unroll
                for (int j = 0; j < 2; ++j)
#pragma unroll
                    for (int tt = 0; tt < 2; ++tt) {
                        const unsigned char* tb = j ? t1 : t0;
                        v2u lo = tr_read(tb + offB(32 * tt + 8 * G + tq_, 2 * (4 * g + wave) + (tp_ >> 1)) + 8 * (tp_ & 1));
                        v2u hi = tr_read(tb + offB(32 * tt + 8 * G + 4 + tq_, 2 * (4 * g + wave) + (tp_ >> 1)) + 8 * (tp_ & 1));
                        wv[4 * (c - 8) + 2 * j + tt] = mk8(lo, hi);
                    }
            } else {
#pragma unroll
                for (int j = 0; j < 2; ++j)
#pragma unroll
                    for (int kk = 0; kk < 2; ++kk)
#pragma unroll
                        for (int half = 0; half < 2; ++half) {
                            const unsigned char* tb = j ? t1 : t0;
                            const int e0 = 32 * kk + 16 * (G & 1) + 4 * (2 * half + (G >> 1)) + tq_;
                            v2u A = tr_read(tb + offB(e0, 4 * wave + (tp_ >> 1)) + 8 * (tp_ & 1));
                            v2u B = tr_read(tb + offB(e0, 4 * wave + 2 + (tp_ >> 1)) + 8 * (tp_ & 1));
                            const int dp = 8 * (c - 12) + 4 * j + wave;
                            *(v4u*)(lds + L_WO + ((dp * 2 + kk) * 2 + half) * 1024 + lane * 16) = (v4u){A.x, B.x, A.y, B.y};
                        }
            }
            asm volatile("s_waitcnt lgkmcnt(0)" ::: "memory");
            __builtin_amdgcn_s_barrier();
        }
    }
    const int sidx = (lane & 8) ? 0xEEEE : 0x4444;
    const int tq = l15 >> 2, tp = lane & 3;
    const unsigned char* memrd = lds + L_MEM + (8 * G + tq) * 16 + (tp & 1) * 8;
    const unsigned char* trd = lds + L_T + (8 * G + tq) * 32 + (tp & 1) * 8;
    const unsigned char* wp = lds + L_WO + wave * 16384 + lane * 16;
    const float sl2e = 0.08838834764831845f * 1.4426950408889634f;
    const bool exist0 = (G < 2);
    constexpr int PD = 4;
    const __amdgpu_buffer_rsrc_t rk = __builtin_amdgcn_make_buffer_rsrc(a.K, 0, S_ * HD * 2, 0x00020000);
    const __amdgpu_buffer_rsrc_t rv = __builtin_amdgcn_make_buffer_rsrc(a.VT, 0, S_ * HD * 2, 0x00020000);
    const __amdgpu_buffer_rsrc_t rkm = __builtin_amdgcn_make_buffer_rsrc(a.KM, 0, NSEG * 8 * HD * 2, 0x00020000);
    const __amdgpu_buffer_rsrc_t rvm = __builtin_amdgcn_make_buffer_rsrc(a.VMT, 0, NSEG * 8 * HD * 2, 0x00020000);

    wait_flag(a.flags + h * FLAG_STRIDE);
    wait_flag(a.flags + (32 + h) * FLAG_STRIDE);
    const unsigned lds0 = __builtin_amdgcn_readfirstlane(lds_addr(lds));
    const u16* ksrc0; const u16* ksrc1; const u16* vsrc;
    {
        const int p0 = tid, p1 = tid + 256;
        ksrc0 = a.K + (size_t)(p0 >> 4) * HD + h * DH + (((p0 & 15) ^ ((p0 >> 4) & 15)) * 8);
        ksrc1 = a.K + (size_t)(p1 >> 4) * HD + h * DH + (((p1 & 15) ^ ((p1 >> 4) & 15)) * 8);
        vsrc = a.VT + ((size_t)h * DH + 64 * g) * 32 + tid * 8;
    }
    auto load_kv = [&](int s) {
        glds16_sc1(ksrc0 + (size_t)s * 32 * HD, lds0 + L_KX + wave * 1024);
        glds16_sc1(ksrc1 + (size_t)s * 32 * HD, lds0 + L_KX + 4096 + wave * 1024);
        glds16_sc1(vsrc + (size_t)s * NH * DH * 32, lds0 + L_VX + wave * 1024);
    };
    v4u kx[2][4]; v2u vx[2];
    auto load_kx = [&](int s) {
#pragma unroll
        for (int kt = 0; kt < 2; ++kt)
#pragma unroll
            for (int ks = 0; ks < 4; ++ks) kx[kt][ks] = __builtin_amdgcn_raw_buffer_load_b128(rk, (unsigned)((s * 32 + kt * 16 + l15) * HD + h * DH + ks * 32 + 8 * G) * 2, 0, 16);
        const unsigned vo = (unsigned)(((s * NH + h) * DH + 64 * g + wave * 16 + l15) * 32 + 4 * G) * 2;
        vx[0] = __builtin_amdgcn_raw_buffer_load_b64(rv, vo, 0, 16); vx[1] = __builtin_amdgcn_raw_buffer_load_b64(rv, vo + 32, 0, 16);
    };
    load_kx(0);
    unsigned long long tpub = 0;
    DIAG_DECL
    for (int s = 0; s < NSEG; ++s) {
        const int sn = (s + 1 < NSEG) ? s + 1 : s;
        {
            v4u m0, m1;
            unsigned spins = 0;
            {
                unsigned sp2 = 0;
                while (__builtin_amdgcn_readfirstlane(__hip_atomic_load(a.flags + (SIG_BASE + 128 + 2 * b) * FLAG_STRIDE, __ATOMIC_RELAXED, __HIP_MEMORY_SCOPE_AGENT)) < (unsigned)s) {
                    if (++sp2 > SPIN_CAP) break;
                }
                asm volatile("" ::: "memory");
            }
            for (;;) {
                m0 = __builtin_amdgcn_raw_buffer_load_b128(rmem, s * MEMX_STEP + tid * 16, 0, 16);
                m1 = __builtin_amdgcn_raw_buffer_load_b128(rmem, s * MEMX_STEP + 4096 + tid * 16, 0, 16);
                *(v4u*)(lds + L_MEM + tid * 16) = m0;
                *(v4u*)(lds + L_MEM + 4096 + tid * 16) = m1;
                unsigned mx = umax3(umax3(m0.x, m0.y, m0.z), umax3(m0.w, m1.x, m1.y), max(m1.z, m1.w));
                asm volatile("" ::: "memory");
                if (__all(mx != 0xFFFFFFFFu)) break;
                if (++spins > SPIN_CAP) break;
            }
        }
        f32x4 acc0 = (f32x4){0.f, 0.f, 0.f, 0.f}, acc1 = acc0;
        asm volatile("" : "+v"(acc0), "+v"(acc1));
        STAMP(0);
        __syncthreads();
        XSIG(0);
        STAMP(1);
        v2u lo[16], hi[16];
#pragma unroll
        for (int t = 0; t < PD; ++t) { lo[t] = tr_read(memrd + t * 512); hi[t] = tr_read(memrd + t * 512 + 64); }
        SB0();
#pragma unroll
        for (int t = 0; t < 16; ++t) {
            if (t + PD < 16) { lo[t + PD] = tr_read(memrd + (t + PD) * 512); hi[t + PD] = tr_read(memrd + (t + PD) * 512 + 64); }
            acc0 = __builtin_amdgcn_smfmac_f32_16x16x64_bf16(mk8(lo[t], hi[t]), wqk[0][t], acc0, sidx, 0, 0);
            acc1 = __builtin_amdgcn_smfmac_f32_16x16x64_bf16(mk8(lo[t], hi[t]), wqk[1][t], acc1, sidx, 0, 0);
            SB0();
        }
        {
            v2u v0 = {pack2(acc0[0], acc0[1]), pack2(acc0[2], acc0[3])};
            v2u v1 = {pack2(acc1[0], acc1[1]), pack2(acc1[2], acc1[3])};
            *(v2u*)(lds + L_T + (wave * 32 + l15) * 32 + G * 8) = v0;
            *(v2u*)(lds + L_T + (wave * 32 + 16 + l15) * 32 + G * 8) = v1;
        }
        STAMP(2);
        __syncthreads();
        XSIG(1);
        STAMP(3);
        v2u q_lo[4], q_hi[4], k_lo[4], k_hi[4];
#pragma unroll
        for (int ks = 0; ks < 4; ++ks) {
            q_lo[ks] = tr_read(trd + ks * 1024); q_hi[ks] = tr_read(trd + ks * 1024 + 128);
            k_lo[ks] = tr_read(trd + ks * 1024 + 16); k_hi[ks] = tr_read(trd + ks * 1024 + 128 + 16);
        }
        f32x4 accv = (f32x4){0.f, 0.f, 0.f, 0.f};
#pragma unroll
        for (int t = 0; t < 4; ++t) accv = mfma16(mk8(lo[t], hi[t]), wv[t], accv);
        SB0();
        f32x4 st[3];
#pragma unroll
        for (int i = 0; i < 3; ++i) st[i] = (f32x4){0.f, 0.f, 0.f, 0.f};
#pragma unroll
        for (int ks = 0; ks < 4; ++ks) {
            bf16x8 qf = mk8(q_lo[ks], q_hi[ks]);
            st[1] = mfma16(as8(kx[0][ks]), qf, st[1]);
            st[2] = mfma16(as8(kx[1][ks]), qf, st[2]);
            st[0] = mfma16(mk8(k_lo[ks], k_hi[ks]), qf, st[0]);
        }
        XSIG(2);
        v4u wo[4][2][2];
#pragma unroll
        for (int i = 0; i < 4; ++i) { wo[i][0][0] = *(const v4u*)(wp + i * 4096); wo[i][0][1] = *(const v4u*)(wp + i * 4096 + 1024); }
        SB0();
#pragma unroll
        for (int t = 4; t < 16; ++t) accv = mfma16(mk8(lo[t], hi[t]), wv[t], accv);
        float m = fmaxf(fmaxf(fmaxf(st[1][0], st[1][1]), fmaxf(st[1][2], st[1][3])), fmaxf(fmaxf(st[2][0], st[2][1]), fmaxf(st[2][2], st[2][3])));
        float m0 = fmaxf(fmaxf(st[0][0], st[0][1]), fmaxf(st[0][2], st[0][3]));
        m = exist0 ? fmaxf(m, m0) : m;
        m = xrow16_max(m);
        const float nm = -m * sl2e;
        float v[3][4];
#pragma unroll
        for (int i = 0; i < 3; ++i)
#pragma unroll
            for (int r = 0; r < 4; ++r) v[i][r] = __builtin_amdgcn_exp2f(__builtin_fmaf(st[i][r], sl2e, nm));
#pragma unroll
        for (int r = 0; r < 4; ++r) v[0][r] = exist0 ? v[0][r] : 0.f;
        v4u p0 = {pack2(v[0][0], v[0][1]), pack2(v[0][2], v[0][3]), pack2(v[1][0], v[1][1]), pack2(v[1][2], v[1][3])};
        v4u p1 = {pack2(v[2][0], v[2][1]), pack2(v[2][2], v[2][3]), 0u, 0u};
        XSIG(5);
        v2u vm = {pack2(accv[0], accv[1]), pack2(accv[2], accv[3])};
        f32x4 o = mfma16(mk8(vx[1], (v2u){0u, 0u}), as8(p1), (f32x4){0.f, 0.f, 0.f, 0.f});
        o = mfma16(mk8(vm, vx[0]), as8(p0), o);
        float sum = ((v[0][0] + v[0][1]) + (v[0][2] + v[0][3])) + ((v[1][0] + v[1][1]) + (v[1][2] + v[1][3])) + ((v[2][0] + v[2][1]) + (v[2][2] + v[2][3]));
        sum = xrow16_sum(sum);
        const float inv = __builtin_amdgcn_rcpf(sum);
        *(v2u*)(lds + L_VM + wave * 512 + lane * 8) = (v2u){pack2(o[0] * inv, o[1] * inv), pack2(o[2] * inv, o[3] * inv)};
        f32x4 d[4];
#pragma unroll
        for (int i = 0; i < 4; ++i) d[i] = (f32x4){0.f, 0.f, 0.f, 0.f};
        asm volatile("" : "+v"(d[0]), "+v"(d[1]), "+v"(d[2]), "+v"(d[3]));
        XSIG(3);
        STAMP(4);
        __syncthreads();
        XSIG(4);
        STAMP(5);
        v2u ob[4];
#pragma unroll
        for (int et = 0; et < 4; ++et) ob[et] = *(const v2u*)(lds + L_VM + et * 512 + lane * 8);
#pragma unroll
        for (int i = 0; i < 4; ++i) { wo[i][1][0] = *(const v4u*)(wp + i * 4096 + 2048); wo[i][1][1] = *(const v4u*)(wp + i * 4096 + 3072); }
        SB0();
        const bf16x8 oa0 = mk8(ob[0], ob[1]), oa1 = mk8(ob[2], ob[3]);
#pragma unroll
        for (int kk = 0; kk < 2; ++kk)
#pragma unroll
            for (int i = 0; i < 4; ++i) {
                bf16x16 wf;
                bf16x8 w0b = as8(wo[i][kk][0]), w1b = as8(wo[i][kk][1]);
#pragma unroll
                for (int j = 0; j < 8; ++j) { wf[j] = w0b[j]; wf[8 + j] = w1b[j]; }
                d[i] = __builtin_amdgcn_smfmac_f32_16x16x64_bf16(kk == 0 ? oa0 : oa1, wf, d[i], sidx, 0, 0);
            }
#pragma unroll
        for (int i = 0; i < 4; ++i) {
            const int dcol = 32 * (wave * 4 + i) + 16 * (G >> 1) + l15;
            v2u pv = {nosent(pack2(d[i][0], d[i][1])), nosent(pack2(d[i][2], d[i][3]))};
            __builtin_amdgcn_raw_buffer_store_b64(pv, rpart, (s & 1) * PART_BUF + (dcol >> 3) * 8192 + b * 128 + (dcol & 7) * 16 + (G & 1) * 8, 0, 16);
        }
        STAMP(6);
        if (G < 2) {
            v2u vs = {nosent(vm.x), nosent(vm.y)};
            __builtin_amdgcn_raw_buffer_store_b64(vs, rvm, (unsigned)(((s * NH + h) * DH + 64 * g + wave * 16 + l15) * 8 + 4 * G) * 2, 0, 16);
        }
        if (g == 0 && l15 < 8) {
            v2u kl = wave == 0 ? k_lo[0] : (wave == 1 ? k_lo[1] : (wave == 2 ? k_lo[2] : k_lo[3]));
            v2u kh = wave == 0 ? k_hi[0] : (wave == 1 ? k_hi[1] : (wave == 2 ? k_hi[2] : k_hi[3]));
            v4u kv = {nosent(kl.x), nosent(kl.y), nosent(kh.x), nosent(kh.y)};
            __builtin_amdgcn_raw_buffer_store_b128(kv, rkm, (unsigned)((s * 8 + l15) * HD + h * DH + wave * 32 + 8 * G) * 2, 0, 16);
        }
        SB0();
        load_kx(sn);
        if ((s & 3) == 2 && s + 2 < NSEG) {
            wait_flag(a.flags + (((s + 2) >> 2) * 96 + h) * FLAG_STRIDE);
            wait_flag(a.flags + (((s + 2) >> 2) * 96 + 32 + h) * FLAG_STRIDE);
        }
    }
    DIAG_END;
}

extern "C" void kernel_launch(void* const* d_in, const int* in_sizes, int n_in, void* d_out, int out_size, void* d_ws, size_t ws_size,
                              hipStream_t stream) {
    const u16* x = (const u16*)d_in[0];
    const u16* mem0 = (const u16*)d_in[1];
    const u16* Wq = (const u16*)d_in[2];
    const u16* Wk = (const u16*)d_in[3];
    const u16* Wv = (const u16*)d_in[4];
    const u16* Wo = (const u16*)d_in[5];
    const u16* Wg = (const u16*)d_in[8];
    const u16* Wu = (const u16*)d_in[9];
    const u16* Wd = (const u16*)d_in[10];
    u16* out = (u16*)d_out;

    unsigned char* ws = (unsigned char*)d_ws;
    size_t off = 0;
    auto alloc = [&](size_t bytes) { unsigned char* p = ws + off; off += (bytes + 255) & ~(size_t)255; return p; };
    constexpr size_t KM_BYTES = (size_t)NSEG * 8 * HD * 2;
    constexpr size_t SENT_BYTES = (size_t)2 * PART_BUF + (size_t)(NSEG + 1) * MEMX_STEP + 2 * KM_BYTES;
    unsigned* flags = (unsigned*)alloc((16 * 96 + 64 + 256) * FLAG_STRIDE * 4);
    unsigned char* xchg = alloc(SENT_BYTES);
    unsigned char* part = xchg;
    unsigned char* memx = xchg + 2 * PART_BUF;
    u16* KM = (u16*)(memx + (size_t)(NSEG + 1) * MEMX_STEP);
    u16* VMT = (u16*)((unsigned char*)KM + KM_BYTES);
    u16* xn = (u16*)alloc((size_t)S_ * DM * 2);
    u16* Q = (u16*)alloc((size_t)S_ * HD * 2);
    u16* K = (u16*)alloc((size_t)S_ * HD * 2);
    u16* VT = (u16*)alloc((size_t)S_ * HD * 2);
    u16* O = (u16*)alloc((size_t)S_ * HD * 2);
    u16* hb = (u16*)alloc((size_t)S_ * DM * 2);
    u16* hn = (u16*)alloc((size_t)S_ * DM * 2);
    u16* act = (u16*)alloc((size_t)S_ * FF * 2);
    float* Pk = (float*)alloc((size_t)8 * S_ * DM * 4);

    k_prep<<<S_ / 4, 256, 0, stream>>>(x, xn, mem0, (u16*)memx, (v4u*)xchg, (int)(SENT_BYTES / 16), (int)(2 * PART_BUF / 16), (v4u*)flags, (16 * 96 + 64 + 256) * FLAG_STRIDE / 4);
    {
        static bool attr_set = false;
        if (!attr_set) { (void)hipFuncSetAttribute((const void*)k_rec, hipFuncAttributeMaxDynamicSharedMemorySize, L_TOTAL); attr_set = true; }
        RecArgs r{Wo, K, VT, Q, O, KM, VMT, memx, part, xn, Wq, Wk, Wv, flags, x, Wg, Wu, Wd, hb, hn, act, out};
        k_rec<<<2 * NX + NWORK, 256, L_TOTAL, stream>>>(r);
    }
    {
        constexpr int ROW0 = RB_IN * 128, MR = S_ - ROW0;
        constexpr int NZ = 8;
        static bool attr2 = false;
        if (!attr2) {
            (void)hipFuncSetAttribute((const void*)k_gemm2<1, 128, 1, 6>, hipFuncAttributeMaxDynamicSharedMemorySize, 6 * (128 * 128 + 8192));
            (void)hipFuncSetAttribute((const void*)k_gemm2<2, 64, 2, 4>, hipFuncAttributeMaxDynamicSharedMemorySize, 4 * (64 * 128 + 16384));
            (void)hipFuncSetAttribute((const void*)k_gemm2<3, 32, 1, 8>, hipFuncAttributeMaxDynamicSharedMemorySize, 8 * (32 * 128 + 8192));
            attr2 = true;
        }
        GemmArgs g1{O, Wo, nullptr, nullptr, nullptr, (u16*)Pk, nullptr, nullptr, HD, DM, HD / NZ, (DM / 64) | (NZ << 16)};
        k_gemm2<1, 128, 1, 6><<<(DM / 64) * (MR / 128) * NZ, 256, 6 * (128 * 128 + 8192), stream>>>(g1, ROW0);
        k_res_ln<NZ><<<MR / 4, 256, 0, stream>>>(Pk, x, hb, hn, ROW0);
        GemmArgs g2{hn, Wg, Wu, nullptr, nullptr, act, nullptr, nullptr, DM, FF, DM, (FF / 64) | (1 << 16)};
        k_gemm2<2, 64, 2, 4><<<(FF / 64) * (MR / 64), 256, 4 * (64 * 128 + 16384), stream>>>(g2, ROW0);
        GemmArgs g3{act, Wd, nullptr, nullptr, hb, out, nullptr, nullptr, FF, DM, FF, (DM / 64) | (1 << 16)};
        k_gemm2<3, 32, 1, 8><<<(DM / 64) * (MR / 32), 256, 8 * (32 * 128 + 8192), stream>>>(g3, ROW0);
    }
}
```

```cpp
#include <hip/hip_runtime.h>
#include <hip/hip_bf16.h>

typedef unsigned short u16;
typedef __bf16 bf16x8 __attribute__((ext_vector_type(8)));
typedef __bf16 bf16x16 __attribute__((ext_vector_type(16)));
typedef float f32x4 __attribute__((ext_vector_type(4)));
typedef unsigned v4u __attribute__((ext_vector_type(4)));
typedef unsigned v2u __attribute__((ext_vector_type(2)));
typedef short v4s __attribute__((ext_vector_type(4)));
#define LAS __attribute__((address_space(3)))

constexpr int S_ = 2048, DM = 512, NH = 32, DH = 128, HD = NH * DH, MEMN = 8, SEG = 32, NSEG = 64, FF = 1792;

__device__ __forceinline__ float bf2f(u16 v) { return __uint_as_float(((unsigned)v) << 16); }
__device__ __forceinline__ u16 f2bf(float f) { return __builtin_bit_cast(u16, (__bf16)f); }
__device__ __forceinline__ float bfr(float f) { return bf2f(f2bf(f)); }
typedef float f32x2 __attribute__((ext_vector_type(2)));
typedef __bf16 bf16x2 __attribute__((ext_vector_type(2)));
__device__ __forceinline__ unsigned pack2(float a, float b) { f32x2 t = {a, b}; return __builtin_bit_cast(unsigned, __builtin_convertvector(t, bf16x2)); }
__device__ __forceinline__ float lo16f(unsigned w) { return __uint_as_float(w << 16); }
__device__ __forceinline__ float hi16f(unsigned w) { return __uint_as_float(w & 0xFFFF0000u); }
template <int CTRL> __device__ __forceinline__ float dppf(float x) {
    return __builtin_bit_cast(float, __builtin_amdgcn_mov_dpp(__builtin_bit_cast(int, x), CTRL, 0xf, 0xf, true));
}
__device__ __forceinline__ float xrow16_max(float x) {
    auto s = __builtin_amdgcn_permlane16_swap(__float_as_uint(x), __float_as_uint(x), false, false);
    x = fmaxf(__uint_as_float(s[0]), __uint_as_float(s[1]));
    auto t = __builtin_amdgcn_permlane32_swap(__float_as_uint(x), __float_as_uint(x), false, false);
    return fmaxf(__uint_as_float(t[0]), __uint_as_float(t[1]));
}
__device__ __forceinline__ float xrow16_sum(float x) {
    auto s = __builtin_amdgcn_permlane16_swap(__float_as_uint(x), __float_as_uint(x), false, false);
    x = __uint_as_float(s[0]) + __uint_as_float(s[1]);
    auto t = __builtin_amdgcn_permlane32_swap(__float_as_uint(x), __float_as_uint(x), false, false);
    return __uint_as_float(t[0]) + __uint_as_float(t[1]);
}
__device__ __forceinline__ unsigned umax3(unsigned a, unsigned b, unsigned c) { return max(max(a, b), c); }
__device__ __forceinline__ unsigned umax4(v4u v) { return max(umax3(v.x, v.y, v.z), v.w); }
__device__ __forceinline__ bool valid16(v4u v) { return umax4(v) != 0xFFFFFFFFu; }
#define SB0() __builtin_amdgcn_sched_barrier(0)
__device__ __forceinline__ void glds16(const void* gsrc, unsigned lds_dst) {
    unsigned keep;
    asm volatile("s_mov_b32 %0, m0\n\ts_mov_b32 m0, %2\n\ts_nop 0\n\tglobal_load_lds_dwordx4 %1, off\n\ts_mov_b32 m0, %0" : "=&s"(keep) : "v"(gsrc), "s"(lds_dst) : "memory");
}
__device__ __forceinline__ void glds16_sc1(const void* gsrc, unsigned lds_dst) {
    unsigned keep;
    asm volatile("s_mov_b32 %0, m0\n\ts_mov_b32 m0, %2\n\ts_nop 0\n\tglobal_load_lds_dwordx4 %1, off sc1\n\ts_mov_b32 m0, %0" : "=&s"(keep) : "v"(gsrc), "s"(lds_dst) : "memory");
}
__device__ __forceinline__ unsigned lds_addr(const void* p) { return (unsigned)(size_t)(LAS const unsigned char*)p; }
__device__ __forceinline__ unsigned nosent(unsigned w) { return min(w, 0xFFFFFFFEu); }
__device__ __forceinline__ v2u tr_read(const void* p) {
    return __builtin_bit_cast(v2u, __builtin_amdgcn_ds_read_tr16_b64_v4i16((LAS v4s*)p));
}
__device__ __forceinline__ bf16x8 mk8(v2u a, v2u b) { v4u t = {a.x, a.y, b.x, b.y}; return __builtin_bit_cast(bf16x8, t); }
__device__ __forceinline__ bf16x8 as8(v4u t) { return __builtin_bit_cast(bf16x8, t); }
__device__ __forceinline__ f32x4 mfma16(bf16x8 a, bf16x8 b, f32x4 c) { return __builtin_amdgcn_mfma_f32_16x16x32_bf16(a, b, c, 0, 0, 0); }

__device__ __forceinline__ float wave_sum(float s) {
    s += dppf<0xB1>(s); s += dppf<0x4E>(s); s += dppf<0x141>(s); s += dppf<0x128>(s);
    return xrow16_sum(s);
}
__device__ __forceinline__ v4u layernorm_vals(v4u raw) {
    float x[8];
#pragma unroll
    for (int i = 0; i < 4; ++i) { x[2 * i] = lo16f(raw[i]); x[2 * i + 1] = hi16f(raw[i]); }
    float s = ((x[0] + x[1]) + (x[2] + x[3])) + ((x[4] + x[5]) + (x[6] + x[7]));
    const float mean = wave_sum(s) * (1.0f / DM);
    float q = 0.f;
#pragma unroll
    for (int i = 0; i < 8; ++i) { float d = x[i] - mean; q += d * d; }
    q = wave_sum(q);
    const float mb = bfr(mean);
    const float vb = bfr(q * (1.0f / DM));
    const float den = bfr(sqrtf(bfr(vb + 1e-5f)));
    v4u outv;
#pragma unroll
    for (int i = 0; i < 4; ++i) {
        float a = bfr(x[2 * i] - mb) / den, b = bfr(x[2 * i + 1] - mb) / den;
        outv[i] = pack2(a, b);
    }
    return outv;
}
__device__ __forceinline__ void layernorm_row(const u16* __restrict__ src, u16* __restrict__ dst, int lane) {
    *(v4u*)(dst + lane * 8) = layernorm_vals(*(const v4u*)(src + lane * 8));
}

__global__ void __launch_bounds__(256) k_prep(const u16* __restrict__ x, u16* __restrict__ xn, const u16* __restrict__ mem0, u16* __restrict__ memx,
                                              v4u* __restrict__ sent, int n_sent16, int memx_off16, v4u* __restrict__ flags, int n_flag16) {
    const int wave = threadIdx.x >> 6, lane = threadIdx.x & 63;
    const int row = blockIdx.x * 4 + wave;
    if (row < S_) layernorm_row(x + (size_t)row * DM, xn + (size_t)row * DM, lane);
    const v4u ones = {0xFFFFFFFFu, 0xFFFFFFFFu, 0xFFFFFFFFu, 0xFFFFFFFFu};
    const int gt = blockIdx.x * 256 + threadIdx.x, nt = gridDim.x * 256;
    for (int i = gt; i < n_sent16; i += nt)
        if (i < memx_off16 || i >= memx_off16 + 512) sent[i] = ones;
    for (int i = gt; i < n_flag16; i += nt) flags[i] = (v4u){0u, 0u, 0u, 0u};
    if (blockIdx.x == 0) {
        for (int i = threadIdx.x; i < MEMN * DM; i += 256) {
            int k = i >> 3, r = i & 7;
            u16 v = mem0[r * DM + k];
            if (v == 0xFFFFu) v = 0x7FFFu;
            memx[i] = v;
        }
    }
}

struct GemmArgs {
    const u16* A; const u16* B0; const u16* B1; const u16* B2; const u16* res;
    u16* C0; u16* C1; u16* C2;
    int lda, ldb, K, pad;
};

__device__ __forceinline__ unsigned offB(unsigned row, unsigned ch) { return 256u * row + 16u * (ch ^ (((row & 3) << 2) | ((row >> 2) & 3))); }
__device__ __forceinline__ unsigned offA(unsigned row, unsigned ch) { return 128u * row + 16u * (ch ^ (row & 7)); }

__device__ __forceinline__ unsigned fB(unsigned row) { return (((row >> 1) & 1) | (((row >> 3) & 1) << 1)) << 1; }
template <int MODE, int BM, int NB, int NST, bool SC1OUT>
__device__ __forceinline__ void gemm_body(const GemmArgs& g, unsigned char* sm, int bm, int bn, int bz) {
    constexpr int MT = BM / 32;
    constexpr int ABYTES = BM * 128, STAGE = ABYTES + NB * 8192;
    constexpr int LA = BM / 32, LPS = LA + 2 * NB;
    const int tid = threadIdx.x, lane = tid & 63;
    const int wave = __builtin_amdgcn_readfirstlane(tid >> 6);
    const int wm = wave >> 1, wn = wave & 1;
    const int kbase = bz * g.K;
    const u16* Asrc[LA];
    const u16* Bsrc[NB][2];
#pragma unroll
    for (int i = 0; i < LA; ++i) { int q = tid + 256 * i; int row = q >> 3, ch = (q & 7) ^ (row & 7); Asrc[i] = g.A + (size_t)(bm + row) * g.lda + kbase + ch * 8; }
#pragma unroll
    for (int n = 0; n < NB; ++n)
#pragma unroll
        for (int i = 0; i < 2; ++i) { int q = tid + 256 * i; int row = q >> 3, ch = (q & 7) ^ fB(row); Bsrc[n][i] = (n == 0 ? g.B0 : g.B1) + (size_t)(kbase + row) * g.ldb + bn + ch * 8; }
    const unsigned sm_base = __builtin_amdgcn_readfirstlane(lds_addr(sm)) + wave * 1024;
    auto stage = [&](int kt) {
        const unsigned base = sm_base + (kt % NST) * STAGE;
#pragma unroll
        for (int i = 0; i < LA; ++i) glds16(Asrc[i] + kt * 64, base + i * 4096);
#pragma unroll
        for (int n = 0; n < NB; ++n)
#pragma unroll
            for (int i = 0; i < 2; ++i) glds16(Bsrc[n][i] + (size_t)kt * 64 * g.ldb, base + ABYTES + n * 8192 + i * 4096);
    };
    f32x4 acc[NB][MT][2];
#pragma unroll
    for (int n = 0; n < NB; ++n)
#pragma unroll
        for (int i = 0; i < MT; ++i) { acc[n][i][0] = (f32x4){0.f, 0.f, 0.f, 0.f}; acc[n][i][1] = acc[n][i][0]; }
    const int nk = g.K / 64;
#pragma unroll
    for (int t = 0; t < NST - 1; ++t) stage(t);
    const int G = lane >> 4, l15 = lane & 15, tq = l15 >> 2, tp = lane & 3;
    for (int kt = 0; kt < nk; ++kt) {
        if (kt + NST - 1 <= nk) asm volatile("s_waitcnt vmcnt(%0)" :: "i"((NST - 2) * LPS) : "memory");
        else asm volatile("s_waitcnt vmcnt(0)" ::: "memory");
        asm volatile("s_waitcnt lgkmcnt(0)" ::: "memory");
        __builtin_amdgcn_s_barrier();
        if (kt + NST - 1 < nk) stage(kt + NST - 1);
        const unsigned char* sA = sm + (kt % NST) * STAGE;
        const unsigned char* sB = sA + ABYTES;
#pragma unroll
        for (int ks = 0; ks < 2; ++ks) {
            bf16x8 af[MT], bq[NB][2];
#pragma unroll
            for (int i = 0; i < MT; ++i) af[i] = as8(*(const v4u*)(sA + offA(wm * (BM / 2) + i * 16 + l15, ks * 4 + G)));
#pragma unroll
            for (int n = 0; n < NB; ++n)
#pragma unroll
                for (int j = 0; j < 2; ++j) {
                    const unsigned r0 = 32 * ks + 8 * G + tq, r1 = r0 + 4, ch = (wn * 2 + j) * 2 + (tp >> 1);
                    v2u lo = tr_read(sB + n * 8192 + r0 * 128 + ((ch ^ fB(r0)) * 16) + 8 * (tp & 1));
                    v2u hi = tr_read(sB + n * 8192 + r1 * 128 + ((ch ^ fB(r1)) * 16) + 8 * (tp & 1));
                    bq[n][j] = mk8(lo, hi);
                }
#pragma unroll
            for (int n = 0; n < NB; ++n)
#pragma unroll
                for (int i = 0; i < MT; ++i)
#pragma unroll
                    for (int j = 0; j < 2; ++j) acc[n][i][j] = mfma16(bq[n][j], af[i], acc[n][i][j]);
        }
    }
#pragma unroll
    for (int i = 0; i < MT; ++i)
#pragma unroll
        for (int j = 0; j < 2; ++j) {
            const int row = bm + wm * (BM / 2) + i * 16 + l15;
            const int col = bn + (wn * 2 + j) * 16 + 4 * G;
            if (MODE == 1) {
                float* P = (float*)g.C0 + ((size_t)bz * S_ + row) * DM + col;
                *(f32x4*)P = acc[0][i][j];
            } else if (MODE == 2) {
                unsigned g0 = pack2(acc[0][i][j][0], acc[0][i][j][1]), g1 = pack2(acc[0][i][j][2], acc[0][i][j][3]);
                unsigned u0 = pack2(acc[NB - 1][i][j][0], acc[NB - 1][i][j][1]), u1 = pack2(acc[NB - 1][i][j][2], acc[NB - 1][i][j][3]);
                v2u v = {pack2(lo16f(g0) * lo16f(u0), hi16f(g0) * hi16f(u0)), pack2(lo16f(g1) * lo16f(u1), hi16f(g1) * hi16f(u1))};
                if (SC1OUT) __hip_atomic_store((unsigned long long*)(g.C0 + (size_t)row * FF + col), __builtin_bit_cast(unsigned long long, v), __ATOMIC_RELAXED, __HIP_MEMORY_SCOPE_AGENT);
                else *(v2u*)(g.C0 + (size_t)row * FF + col) = v;
            } else {
                v2u r = *(const v2u*)(g.res + (size_t)row * DM + col);
                unsigned a0 = pack2(acc[0][i][j][0], acc[0][i][j][1]), a1 = pack2(acc[0][i][j][2], acc[0][i][j][3]);
                v2u v = {pack2(lo16f(r.x) + lo16f(a0), hi16f(r.x) + hi16f(a0)), pack2(lo16f(r.y) + lo16f(a1), hi16f(r.y) + hi16f(a1))};
                if (SC1OUT) __hip_atomic_store((unsigned long long*)(g.C0 + (size_t)row * DM + col), __builtin_bit_cast(unsigned long long, v), __ATOMIC_RELAXED, __HIP_MEMORY_SCOPE_AGENT);
                else *(v2u*)(g.C0 + (size_t)row * DM + col) = v;
            }
        }
}

template <int MODE, int BM, int NB, int NST>
__global__ void __launch_bounds__(256) k_gemm2(GemmArgs g, int row0) {
    extern __shared__ __attribute__((aligned(16))) unsigned char sm[];
    const int ncol = g.pad & 0xFFFF, nz = g.pad >> 16;
    const int per_xcd = gridDim.x >> 3;
    const int lin = (blockIdx.x & 7) * per_xcd + (blockIdx.x >> 3);
    const int panel = lin / ncol;
    gemm_body<MODE, BM, NB, NST, false>(g, sm, row0 + (panel / nz) * BM, (lin % ncol) * 64, panel % nz);
}

template <int NZ>
__global__ void __launch_bounds__(256) k_res_ln(const float* __restrict__ P, const u16* __restrict__ x, u16* __restrict__ hb, u16* __restrict__ hn, int row0) {
    const int wave = threadIdx.x >> 6, lane = threadIdx.x & 63;
    const int row = row0 + blockIdx.x * 4 + wave;
    f32x4 s0 = *(const f32x4*)(P + (size_t)row * DM + lane * 8), s1 = *(const f32x4*)(P + (size_t)row * DM + lane * 8 + 4);
#pragma unroll
    for (int z = 1; z < NZ; ++z) { s0 += *(const f32x4*)(P + ((size_t)z * S_ + row) * DM + lane * 8); s1 += *(const f32x4*)(P + ((size_t)z * S_ + row) * DM + lane * 8 + 4); }
    v4u raw = *(const v4u*)(x + (size_t)row * DM + lane * 8);
    unsigned a0 = pack2(s0[0], s0[1]), a1 = pack2(s0[2], s0[3]), a2 = pack2(s1[0], s1[1]), a3 = pack2(s1[2], s1[3]);
    v4u hv = {pack2(lo16f(raw.x) + lo16f(a0), hi16f(raw.x) + hi16f(a0)), pack2(lo16f(raw.y) + lo16f(a1), hi16f(raw.y) + hi16f(a1)),
              pack2(lo16f(raw.z) + lo16f(a2), hi16f(raw.z) + hi16f(a2)), pack2(lo16f(raw.w) + lo16f(a3), hi16f(raw.w) + hi16f(a3))};
    *(v4u*)(hb + (size_t)row * DM + lane * 8) = hv;
    *(v4u*)(hn + (size_t)row * DM + lane * 8) = layernorm_vals(hv);
}

#define KX_DMA 0
#define XDELAY 0
#define YDELAY 0
#define YWAIT 6
#define XWAIT 0
#define YSIG 1
#define SIGPOS 4
#define WOEARLY 0
#define PINA 0
#define YMULTI 0
#define YSTAG 12
#define YADAPT 0
#define YINIT 0
#define YDN 4
#define YUP 12
constexpr int NX = 64;
constexpr int MEMX_STEP = 8192;
constexpr int PART_BUF = 64 * 64 * 128;
constexpr unsigned SPIN_CAP = 1u << 22;

#define WSTAMP() do {} while (0)
#define STAMP(i) do {} while (0)
#define DIAG_DECL
#define DIAG_END
struct RecArgs {
    const u16* Wo;
    u16* K; u16* VT; u16* Q; u16* O;
    u16* KM; u16* VMT;
    unsigned char* memx; unsigned char* part;
    const u16* xn; const u16* Wq; const u16* Wk; const u16* Wv;
    unsigned* flags;
    const u16* x; const u16* Wg; const u16* Wu; const u16* Wd;
    u16* hb; u16* hn; u16* act; u16* out;
};
#define RBIN 12
#define RBW 13
#define RBF 13
constexpr int RB_W = RBW, RB_F1 = RBF;
constexpr int RB_IN = RBIN;
constexpr int SIG_BASE = 16 * 96 + 64;
constexpr int CNT_BASE = 16 * 96;
constexpr int NWORK = 128;
constexpr int FLAG_STRIDE = 16;

__device__ __forceinline__ void wait_flag(const unsigned* f) {
    unsigned spins = 0;
    while (__builtin_amdgcn_readfirstlane(__hip_atomic_load(f, __ATOMIC_RELAXED, __HIP_MEMORY_SCOPE_AGENT)) == 0u) {
        __builtin_amdgcn_s_sleep(2);
        if (++spins > SPIN_CAP) break;
    }
    asm volatile("" ::: "memory");
}

__device__ __forceinline__ void wait_count_acquire(const unsigned* c, unsigned target) {
    if (threadIdx.x < 64) {
        unsigned spins = 0;
        while (__builtin_amdgcn_readfirstlane(__hip_atomic_load(c, __ATOMIC_RELAXED, __HIP_MEMORY_SCOPE_AGENT)) < target) {
            __builtin_amdgcn_s_sleep(32);
            if (++spins > SPIN_CAP) break;
        }
        __builtin_amdgcn_fence(__ATOMIC_ACQUIRE, "agent");
        asm volatile("s_waitcnt vmcnt(0)" ::: "memory");
    }
    __syncthreads();
}
__device__ __forceinline__ void signal_count(unsigned* c) {
    asm volatile("s_waitcnt vmcnt(0)" ::: "memory");
    __syncthreads();
    if (threadIdx.x == 0) __hip_atomic_fetch_add(c, 1u, __ATOMIC_RELAXED, __HIP_MEMORY_SCOPE_AGENT);
}

template <bool ISV>
__device__ __forceinline__ void proj_col(const RecArgs& a, unsigned char* lds, int c) {
    constexpr int NST = 8;
    const int tid = threadIdx.x, lane = tid & 63;
    const int wave = __builtin_amdgcn_readfirstlane(tid >> 6);
    const int wm = wave >> 1, wn = wave & 1;
    const int mat = c >> 5, head = c & 31;
    const int G = lane >> 4, l15 = lane & 15, tq = l15 >> 2, tp = lane & 3;
    const unsigned sbase = __builtin_amdgcn_readfirstlane(lds_addr(lds)) + wave * 1024;
    bf16x8 bw[4][16];
    {
        const u16* Bp = (mat == 0 ? a.Wk : (mat == 1 ? a.Wv : a.Wq)) + head * DH;
#pragma unroll
        for (int kt = 0; kt < 8; ++kt)
#pragma unroll
            for (int i = 0; i < 4; ++i) {
                const int q = tid + 256 * i, r = q >> 4;
                glds16(Bp + (size_t)(kt * 64 + r) * HD + (((q & 15) ^ (((r & 3) << 2) | ((r >> 2) & 3))) * 8), sbase + kt * 16384 + i * 4096);
            }
        asm volatile("s_waitcnt vmcnt(0)" ::: "memory");
        __builtin_amdgcn_s_barrier();
#pragma unroll
        for (int t = 0; t < 16; ++t)
#pragma unroll
            for (int j = 0; j < 4; ++j) {
                const unsigned char* sB = lds + (t >> 1) * 16384;
                const int cc = wn * 4 + j, ks = t & 1;
                v2u lo = tr_read(sB + offB(32 * ks + 8 * G + tq, 2 * cc + (tp >> 1)) + 8 * (tp & 1));
                v2u hi = tr_read(sB + offB(32 * ks + 8 * G + 4 + tq, 2 * cc + (tp >> 1)) + 8 * (tp & 1));
                bw[j][t] = mk8(lo, hi);
            }
        asm volatile("s_waitcnt lgkmcnt(0)" ::: "memory");
        __builtin_amdgcn_s_barrier();
    }
    const u16* asrc[4];
#pragma unroll
    for (int i = 0; i < 4; ++i) { const int q = tid + 256 * i; asrc[i] = a.xn + (size_t)(q >> 3) * DM + (((q & 7) ^ ((q >> 3) & 7)) * 8); }
    auto stage = [&](int n) {
        const unsigned d = sbase + (n & (NST - 1)) * 16384;
        const size_t off = (size_t)(n >> 3) * 128 * DM + (n & 7) * 64;
#pragma unroll
        for (int i = 0; i < 4; ++i) glds16(asrc[i] + off, d + i * 4096);
    };
#pragma unroll
    for (int n = 0; n < NST - 1; ++n) stage(n);
    unsigned char* img = lds + 131072;
#pragma unroll 1
    for (int rb = 0; rb < 16; ++rb) {
        f32x4 acc[4][4];
#pragma unroll
        for (int i = 0; i < 4; ++i)
#pragma unroll
            for (int j = 0; j < 4; ++j) acc[i][j] = (f32x4){0.f, 0.f, 0.f, 0.f};
#pragma unroll
        for (int kt = 0; kt < 8; ++kt) {
            const int n = rb * 8 + kt;
            if (n + NST - 1 > 128) asm volatile("s_waitcnt vmcnt(0)" ::: "memory");
            else if (kt < 7 && rb > 0) asm volatile("s_waitcnt vmcnt(32)" ::: "memory");
            else asm volatile("s_waitcnt vmcnt(24)" ::: "memory");
            asm volatile("s_waitcnt lgkmcnt(0)" ::: "memory");
            __builtin_amdgcn_s_barrier();
            if (kt == 7 && rb >= 2 && tid == 0) __hip_atomic_store(a.flags + ((rb - 1) * 96 + c) * FLAG_STRIDE, 1u, __ATOMIC_RELAXED, __HIP_MEMORY_SCOPE_AGENT);
            if (n + NST - 1 < 128) stage(n + NST - 1);
            const unsigned char* sA = lds + kt * 16384;
#pragma unroll
            for (int ks = 0; ks < 2; ++ks) {
                bf16x8 af[4];
#pragma unroll
                for (int i = 0; i < 4; ++i) af[i] = as8(*(const v4u*)(sA + offA(wm * 64 + i * 16 + l15, ks * 4 + G)));
#pragma unroll
                for (int i = 0; i < 4; ++i)
#pragma unroll
                    for (int j = 0; j < 4; ++j) acc[i][j] = ISV ? mfma16(af[i], bw[j][kt * 2 + ks], acc[i][j]) : mfma16(bw[j][kt * 2 + ks], af[i], acc[i][j]);
            }
        }
#pragma unroll
        for (int i = 0; i < 4; ++i)
#pragma unroll
            for (int j = 0; j < 4; ++j) {
                v2u v = {pack2(acc[i][j][0], acc[i][j][1]), pack2(acc[i][j][2], acc[i][j][3])};
                if (ISV) {
                    const int e = (wn * 4 + j) * 16 + l15, row0 = wm * 64 + i * 16 + 4 * G;
                    const int line = (row0 >> 5) * 128 + e;
                    *(v2u*)(img + line * 64 + ((((row0 & 31) >> 2) ^ (e & 7)) * 8)) = v;
                } else {
                    const int row = wm * 64 + i * 16 + l15, col0 = (wn * 4 + j) * 16 + 4 * G;
                    *(v2u*)(img + row * 256 + (((col0 >> 3) ^ (row & 15)) * 16) + ((col0 & 4) * 2)) = v;
                }
            }
        asm volatile("s_waitcnt lgkmcnt(0)" ::: "memory");
        __builtin_amdgcn_s_barrier();
        if (ISV) {
            const __amdgpu_buffer_rsrc_t r = __builtin_amdgcn_make_buffer_rsrc(a.VT, 0, S_ * HD * 2, 0x00020000);
#pragma unroll
            for (int i = 0; i < 8; ++i) {
                const int q = tid + 256 * i;
                const int line = q >> 2, cq = q & 3, e = line & 127;
                v2u x0 = *(const v2u*)(img + line * 64 + (((2 * cq) ^ (e & 7)) * 8));
                v2u x1 = *(const v2u*)(img + line * 64 + (((2 * cq + 1) ^ (e & 7)) * 8));
                v4u v = {x0.x, x0.y, x1.x, x1.y};
                const int seg = rb * 4 + (line >> 7);
                __builtin_amdgcn_raw_buffer_store_b128(v, r, (unsigned)((((seg * NH + head) * DH + e) * 32 + cq * 8) * 2), 0, 16);
            }
        } else {
            const __amdgpu_buffer_rsrc_t r = __builtin_amdgcn_make_buffer_rsrc(mat == 0 ? a.K : a.Q, 0, S_ * HD * 2, 0x00020000);
#pragma unroll
            for (int i = 0; i < 8; ++i) {
                const int q = tid + 256 * i;
                const int row = q >> 4, ch = q & 15;
                v4u v = *(const v4u*)(img + row * 256 + ((ch ^ (row & 15)) * 16));
                __builtin_amdgcn_raw_buffer_store_b128(v, r, (unsigned)(((rb * 128 + row) * HD + head * DH + ch * 8) * 2), 0, 16);
            }
        }
        if (rb == 0 || rb == 15) {
            asm volatile("s_waitcnt vmcnt(0) lgkmcnt(0)" ::: "memory");
            __builtin_amdgcn_s_barrier();
            if (tid == 0) __hip_atomic_store(a.flags + (rb * 96 + c) * FLAG_STRIDE, 1u, __ATOMIC_RELAXED, __HIP_MEMORY_SCOPE_AGENT);
        }
    }
    asm volatile("s_waitcnt vmcnt(0)" ::: "memory");
    __syncthreads();
}

__device__ __forceinline__ void attn_unit(const RecArgs& a, int seg, int h, int lane) {
    const int G = lane >> 4, l15 = lane & 15;
    const float scale = 0.08838834764831845f;
    const __amdgpu_buffer_rsrc_t rq = __builtin_amdgcn_make_buffer_rsrc(a.Q, 0, S_ * HD * 2, 0x00020000);
    const __amdgpu_buffer_rsrc_t rk = __builtin_amdgcn_make_buffer_rsrc(a.K, 0, S_ * HD * 2, 0x00020000);
    const __amdgpu_buffer_rsrc_t rv = __builtin_amdgcn_make_buffer_rsrc(a.VT, 0, S_ * HD * 2, 0x00020000);
    const __amdgpu_buffer_rsrc_t rkm = __builtin_amdgcn_make_buffer_rsrc(a.KM, 0, NSEG * 8 * HD * 2, 0x00020000);
    const __amdgpu_buffer_rsrc_t rvm = __builtin_amdgcn_make_buffer_rsrc(a.VMT, 0, NSEG * 8 * HD * 2, 0x00020000);
    const int rb = seg >> 2;
    wait_flag(a.flags + (rb * 96 + h) * FLAG_STRIDE);
    wait_flag(a.flags + (rb * 96 + 32 + h) * FLAG_STRIDE);
    wait_flag(a.flags + (rb * 96 + 64 + h) * FLAG_STRIDE);
    v4u k1[4], k2[4], q0[4], q1[4];
    v2u v1[8], v2[8];
#pragma unroll
    for (int ks = 0; ks < 4; ++ks) {
        const unsigned eo = (unsigned)(h * DH + ks * 32 + 8 * G) * 2;
        k1[ks] = __builtin_amdgcn_raw_buffer_load_b128(rk, (unsigned)(seg * 32 + l15) * (HD * 2) + eo, 0, 16);
        k2[ks] = __builtin_amdgcn_raw_buffer_load_b128(rk, (unsigned)(seg * 32 + 16 + l15) * (HD * 2) + eo, 0, 16);
        q0[ks] = __builtin_amdgcn_raw_buffer_load_b128(rq, (unsigned)(seg * 32 + l15) * (HD * 2) + eo, 0, 16);
        q1[ks] = __builtin_amdgcn_raw_buffer_load_b128(rq, (unsigned)(seg * 32 + 16 + l15) * (HD * 2) + eo, 0, 16);
    }
#pragma unroll
    for (int et = 0; et < 8; ++et) {
        const unsigned vo = (unsigned)(((seg * NH + h) * DH + et * 16 + l15) * 32 + 4 * G) * 2;
        v1[et] = __builtin_amdgcn_raw_buffer_load_b64(rv, vo, 0, 16);
        v2[et] = __builtin_amdgcn_raw_buffer_load_b64(rv, vo + 32, 0, 16);
    }
    v4u k0[4]; v2u vm[8];
    {
        unsigned spins = 0;
        const unsigned* probe = (const unsigned*)(a.VMT + ((size_t)(seg * NH + h) * DH + 127) * 8);
        while (__builtin_amdgcn_readfirstlane(__hip_atomic_load(probe, __ATOMIC_RELAXED, __HIP_MEMORY_SCOPE_AGENT)) == 0xFFFFFFFFu) {
            if (seg >= NSEG - 2) __builtin_amdgcn_s_sleep(10); else __builtin_amdgcn_s_sleep(100);
            if (++spins > SPIN_CAP) break;
        }
        for (;;) {
            unsigned mx = 0;
#pragma unroll
            for (int ks = 0; ks < 4; ++ks) {
                k0[ks] = __builtin_amdgcn_raw_buffer_load_b128(rkm, (unsigned)((seg * 8 + (l15 & 7)) * HD + h * DH + ks * 32 + 8 * G) * 2, 0, 16);
            }
#pragma unroll
            for (int et = 0; et < 8; ++et) vm[et] = __builtin_amdgcn_raw_buffer_load_b64(rvm, (unsigned)(((seg * NH + h) * DH + et * 16 + l15) * 8 + 4 * (G & 1)) * 2, 0, 16);
#pragma unroll
            for (int ks = 0; ks < 4; ++ks) mx = max(mx, umax4(k0[ks]));
#pragma unroll
            for (int et = 0; et < 8; ++et) mx = umax3(mx, vm[et].x, vm[et].y);
            asm volatile("" ::: "memory");
            if (__all(mx != 0xFFFFFFFFu)) break;
            __builtin_amdgcn_s_sleep(16);
            if (++spins > SPIN_CAP) break;
        }
    }
    f32x4 st[3][2];
#pragma unroll
    for (int i = 0; i < 3; ++i) { st[i][0] = (f32x4){0.f, 0.f, 0.f, 0.f}; st[i][1] = st[i][0]; }
#pragma unroll
    for (int ks = 0; ks < 4; ++ks) {
        st[0][0] = mfma16(as8(k0[ks]), as8(q0[ks]), st[0][0]); st[0][1] = mfma16(as8(k0[ks]), as8(q1[ks]), st[0][1]);
        st[1][0] = mfma16(as8(k1[ks]), as8(q0[ks]), st[1][0]); st[1][1] = mfma16(as8(k1[ks]), as8(q1[ks]), st[1][1]);
        st[2][0] = mfma16(as8(k2[ks]), as8(q0[ks]), st[2][0]); st[2][1] = mfma16(as8(k2[ks]), as8(q1[ks]), st[2][1]);
    }
    bf16x8 pf[2][2];
#pragma unroll
    for (int qt = 0; qt < 2; ++qt) {
        const int t = qt * 16 + l15;
        float v[3][4];
        float m = -3.0e38f;
#pragma unroll
        for (int i = 0; i < 3; ++i)
#pragma unroll
            for (int r = 0; r < 4; ++r) {
                float sc = bfr(bfr(st[i][qt][r]) * scale);
                bool ok;
                if (i == 0) ok = (G < 2); else ok = ((i - 1) * 16 + 4 * G + r) <= t;
                bool exist = (i != 0) || (G < 2);
                sc = ok ? sc : bfr(-1.0e9f);
                v[i][r] = exist ? sc : -3.0e38f;
                m = fmaxf(m, v[i][r]);
            }
        m = xrow16_max(m);
        float sum = 0.f;
#pragma unroll
        for (int i = 0; i < 3; ++i)
#pragma unroll
            for (int r = 0; r < 4; ++r) { bool exist = (i != 0) || (G < 2); v[i][r] = exist ? __expf(v[i][r] - m) : 0.f; sum += v[i][r]; }
        sum = xrow16_sum(sum);
        const float inv = 1.0f / sum;
        v4u p0 = {pack2(v[0][0] * inv, v[0][1] * inv), pack2(v[0][2] * inv, v[0][3] * inv), pack2(v[1][0] * inv, v[1][1] * inv), pack2(v[1][2] * inv, v[1][3] * inv)};
        v4u p1 = {pack2(v[2][0] * inv, v[2][1] * inv), pack2(v[2][2] * inv, v[2][3] * inv), 0u, 0u};
        pf[qt][0] = as8(p0); pf[qt][1] = as8(p1);
    }
#pragma unroll
    for (int et = 0; et < 8; ++et) {
        bf16x8 a0 = mk8(vm[et], v1[et]);
        bf16x8 a1 = mk8(v2[et], (v2u){0u, 0u});
#pragma unroll
        for (int qt = 0; qt < 2; ++qt) {
            f32x4 o = (f32x4){0.f, 0.f, 0.f, 0.f};
            o = mfma16(a0, pf[qt][0], o);
            o = mfma16(a1, pf[qt][1], o);
            v2u ov = {pack2(o[0], o[1]), pack2(o[2], o[3])};
            __hip_atomic_store((unsigned long long*)(a.O + ((size_t)seg * 32 + qt * 16 + l15) * HD + h * DH + et * 16 + 4 * G), __builtin_bit_cast(unsigned long long, ov), __ATOMIC_RELAXED, __HIP_MEMORY_SCOPE_AGENT);
        }
    }
}

constexpr int L_WO = 0;
constexpr int L_MEM = 65536;
constexpr int L_T = L_MEM + 8192;
constexpr int L_VM = L_T + 4096;
constexpr int L_KX = L_VM + 2048;
constexpr int L_VX = L_KX + 8192;
constexpr int L_STG = 96 * 1024;
constexpr int L_TOTAL = 160 * 1024;

#define XSIG(pos) do { if (SIGPOS == (pos) && tid == 0) __hip_atomic_store(a.flags + (SIG_BASE + 2 * b) * FLAG_STRIDE, (unsigned)(s + 1), __ATOMIC_RELAXED, __HIP_MEMORY_SCOPE_AGENT); } while (0)
__global__ void __launch_bounds__(256, 1) k_rec(RecArgs a) {
    extern __shared__ __attribute__((aligned(16))) unsigned char lds[];
    const int tid = threadIdx.x, lane = tid & 63;
    const int wave = __builtin_amdgcn_readfirstlane(tid >> 6);
    const int G = lane >> 4, l15 = lane & 15;
    const __amdgpu_buffer_rsrc_t rmem = __builtin_amdgcn_make_buffer_rsrc(a.memx, 0, (NSEG + 1) * MEMX_STEP, 0x00020000);
    const __amdgpu_buffer_rsrc_t rpart = __builtin_amdgcn_make_buffer_rsrc(a.part, 0, 2 * PART_BUF, 0x00020000);

#define XCDMAP 0
    const int bid = blockIdx.x;
    if (bid >= 2 * NX) {
        const int w = bid - 2 * NX;
        int wsn_ = 0; (void)wsn_;
        WSTAMP();
        if (w >= 32) {
            const int c = w - 32;
            if ((c >> 5) == 1) proj_col<true>(a, lds, c); else proj_col<false>(a, lds, c);
        }
        WSTAMP();
        if (w < 32) {
            for (int rb = 0; rb < 16; ++rb) {
                attn_unit(a, rb * 4 + (w >> 3), (w & 7) * 4 + wave, lane);
                signal_count(a.flags + (CNT_BASE + rb * 4) * FLAG_STRIDE);
                WSTAMP();
            }
        } else if (w < 80) {
            const int t = (w - 32) & 15;
            for (int rb = (w - 32) >> 4; rb < RB_W; rb += 3) {
                unsigned* cnt = a.flags + (CNT_BASE + rb * 4) * FLAG_STRIDE;
                wait_count_acquire(cnt, 32);
                WSTAMP();
                GemmArgs g{a.O, a.Wo, nullptr, nullptr, a.x, a.hb, nullptr, nullptr, HD, DM, HD, 0};
                gemm_body<3, 64, 1, 8, true>(g, lds, rb * 128 + (t >> 3) * 64, (t & 7) * 64, 0);
                signal_count(cnt + FLAG_STRIDE);
                WSTAMP();
                wait_count_acquire(cnt + FLAG_STRIDE, 16);
                const __amdgpu_buffer_rsrc_t rh = __builtin_amdgcn_make_buffer_rsrc(a.hn, 0, S_ * DM * 2, 0x00020000);
                const int row0 = rb * 128 + t * 8 + wave * 2;
                v4u h0 = *(const v4u*)(a.hb + (size_t)row0 * DM + lane * 8), h1 = *(const v4u*)(a.hb + (size_t)(row0 + 1) * DM + lane * 8);
                __builtin_amdgcn_raw_buffer_store_b128(layernorm_vals(h0), rh, (unsigned)(row0 * DM + lane * 8) * 2, 0, 16);
                __builtin_amdgcn_raw_buffer_store_b128(layernorm_vals(h1), rh, (unsigned)((row0 + 1) * DM + lane * 8) * 2, 0, 16);
                signal_count(cnt + 2 * FLAG_STRIDE);
            }
        } else if (w < 112) {
            for (int u = w - 80; u < RB_F1 * 28; u += 32) {
                const int rb = u / 28;
                unsigned* cnt = a.flags + (CNT_BASE + rb * 4) * FLAG_STRIDE;
                wait_count_acquire(cnt + 2 * FLAG_STRIDE, 16);
                WSTAMP();
                GemmArgs g{a.hn, a.Wg, a.Wu, nullptr, nullptr, a.act, nullptr, nullptr, DM, FF, DM, 0};
                gemm_body<2, 128, 2, 4, true>(g, lds, rb * 128, (u % 28) * 64, 0);
                signal_count(cnt + 3 * FLAG_STRIDE);
                WSTAMP();
            }
        } else {
            for (int rb = (w - 112) >> 3; rb < RB_IN; rb += 2) {
                unsigned* cnt = a.flags + (CNT_BASE + rb * 4) * FLAG_STRIDE;
                wait_count_acquire(cnt + 3 * FLAG_STRIDE, 28);
                WSTAMP();
                GemmArgs g{a.act, a.Wd, nullptr, nullptr, a.hb, a.out, nullptr, nullptr, FF, DM, FF, 0};
                gemm_body<3, 128, 1, 6, false>(g, lds, rb * 128, (w & 7) * 64, 0);
                __syncthreads();
                WSTAMP();
            }
        }
        return;
    }
    if (bid >= NX) {
        if (wave != 0) return;
        const int y = bid - NX;
        DIAG_DECL
        unsigned long long typub = 0;
        unsigned ydel = YINIT, ydmin = 1000u; (void)ydel; (void)ydmin;
        bf16x8 sel;
#pragma unroll
        for (int j = 0; j < 8; ++j) sel[j] = (l15 == j) ? (__bf16)1.0f : (__bf16)0.0f;
        for (int s = 0; s < NSEG - 1; ++s) {
            const unsigned base = (s & 1) * PART_BUF + y * 8192 + lane * 16;
            v4u m[8];
            unsigned spins = 0;
            {
                unsigned sp2 = 0;
                while (__builtin_amdgcn_readfirstlane(__hip_atomic_load(a.flags + (SIG_BASE + 2 * y) * FLAG_STRIDE, __ATOMIC_RELAXED, __HIP_MEMORY_SCOPE_AGENT)) < (unsigned)(s + 1)) {
                    if (++sp2 > SPIN_CAP) break;
                }
                __builtin_amdgcn_s_sleep(YWAIT);
                asm volatile("" ::: "memory");
            }
            f32x4 acc0, acc1;
            for (;;) {
#pragma unroll
                for (int t = 0; t < 8; ++t) m[t] = __builtin_amdgcn_raw_buffer_load_b128(rpart, base + t * 1024, 0, 16);
                unsigned mx = 0;
                acc0 = (f32x4){0.f, 0.f, 0.f, 0.f}; acc1 = acc0;
#pragma unroll
                for (int t = 0; t < 8; t += 2) {
                    acc0 = mfma16(sel, as8(m[t]), acc0); acc1 = mfma16(sel, as8(m[t + 1]), acc1);
                    mx = umax3(mx, umax3(m[t].x, m[t].y, m[t].z), m[t].w);
                    mx = umax3(mx, umax3(m[t + 1].x, m[t + 1].y, m[t + 1].z), m[t + 1].w);
                }
                asm volatile("" : "+v"(acc0), "+v"(acc1) :: "memory");
                if (__all(mx != 0xFFFFFFFFu)) break;
                if (++spins > SPIN_CAP) break;
            }
            if (lane == 0) __hip_atomic_store(a.flags + (SIG_BASE + 128 + 2 * y) * FLAG_STRIDE, (unsigned)(s + 1), __ATOMIC_RELAXED, __HIP_MEMORY_SCOPE_AGENT);
            STAMP(0);
            acc0 += acc1;
#pragma unroll
            for (int r = 0; r < 4; ++r) acc0[r] += dppf<0x128>(acc0[r]);
            if (lane < 32 && l15 < 8) {
                v2u v = {nosent(pack2(acc0[0], acc0[1])), nosent(pack2(acc0[2], acc0[3]))};
                __builtin_amdgcn_raw_buffer_store_b64(v, rmem, (s + 1) * MEMX_STEP + y * 128 + l15 * 16 + G * 8, 0, 16);
            }
            STAMP(1);
            const v4u sent = {0xFFFFFFFFu, 0xFFFFFFFFu, 0xFFFFFFFFu, 0xFFFFFFFFu};
#pragma unroll
            for (int t = 0; t < 8; ++t) __builtin_amdgcn_raw_buffer_store_b128(sent, rpart, base + t * 1024, 0, 16);
            asm volatile("s_waitcnt vmcnt(0)" ::: "memory");
        }
        DIAG_END;
        return;
    }

    const int b = bid, h = b >> 1, g = b & 1;
    bf16x16 wqk[2][16];
    bf16x8 wv[16];
    {
        const int tq_ = l15 >> 2, tp_ = lane & 3;
        const unsigned stg = __builtin_amdgcn_readfirstlane(lds_addr(lds)) + wave * 1024;
        int srow[4], sch[4];
#pragma unroll
        for (int i = 0; i < 4; ++i) { const int q = tid + 256 * i; srow[i] = q >> 4; sch[i] = ((q & 15) ^ (((srow[i] & 3) << 2) | ((srow[i] >> 2) & 3))) * 8; }
        auto issue = [&](int c) {
            const u16* s0; const u16* s1; int ld;
            if (c < 8) { s0 = a.Wq + (size_t)(64 * c) * HD + h * DH; s1 = a.Wk + (size_t)(64 * c) * HD + h * DH; ld = HD; }
            else if (c < 12) { s0 = a.Wv + (size_t)(128 * (c - 8)) * HD + h * DH; s1 = s0 + (size_t)64 * HD; ld = HD; }
            else { s0 = a.Wo + (size_t)(h * DH + 64 * g) * DM + 256 * (c - 12); s1 = s0 + 128; ld = DM; }
            const unsigned dst = stg + (((c & 3) < 2) ? (unsigned)(L_STG + (c & 1) * 32768) : (unsigned)(L_WO + (c & 1) * 32768));
#pragma unroll
            for (int i = 0; i < 4; ++i) glds16(s0 + (size_t)srow[i] * ld + sch[i], dst + i * 4096);
#pragma unroll
            for (int i = 0; i < 4; ++i) glds16(s1 + (size_t)srow[i] * ld + sch[i], dst + 16384 + i * 4096);
        };
        issue(0); issue(1); issue(2);
#pragma unroll
        for (int c = 0; c < 14; ++c) {
            if (c + 3 < 14) { issue(c + 3); asm volatile("s_waitcnt vmcnt(24)" ::: "memory"); }
            else if (c + 3 == 14) asm volatile("s_waitcnt vmcnt(16)" ::: "memory");
            else if (c + 2 == 14) asm volatile("s_waitcnt vmcnt(8)" ::: "memory");
            else asm volatile("s_waitcnt vmcnt(0)" ::: "memory");
            __builtin_amdgcn_s_barrier();
            const unsigned char* t0 = lds + (((c & 3) < 2) ? (L_STG + (c & 1) * 32768) : (L_WO + (c & 1) * 32768));
            const unsigned char* t1 = t0 + 16384;
            if (c < 8) {
#pragma unroll
                for (int tt = 0; tt < 2; ++tt)
#pragma unroll
                    for (int u = 0; u < 2; ++u) {
                        v4u hf[2];
#pragma unroll
                        for (int half = 0; half < 2; ++half) {
                            const unsigned off = offB(32 * tt + 16 * half + 4 * G + tq_, 2 * (2 * wave + u) + (tp_ >> 1)) + 8 * (tp_ & 1);
                            v2u q = tr_read(t0 + off), k = tr_read(t1 + off);
                            hf[half] = (v4u){q.x, k.x, q.y, k.y};
                        }
                        bf16x8 lo = as8(hf[0]), hi = as8(hf[1]);
#pragma unroll
                        for (int j = 0; j < 8; ++j) { wqk[u][2 * c + tt][j] = lo[j]; wqk[u][2 * c + tt][8 + j] = hi[j]; }
                    }
            } else if (c < 12) {
#pragma unroll
                for (int j = 0; j < 2; ++j)
#pragma unroll
                    for (int tt = 0; tt < 2; ++tt) {
                        const unsigned char* tb = j ? t1 : t0;
                        v2u lo = tr_read(tb + offB(32 * tt + 8 * G + tq_, 2 * (4 * g + wave) + (tp_ >> 1)) + 8 * (tp_ & 1));
                        v2u hi = tr_read(tb + offB(32 * tt + 8 * G + 4 + tq_, 2 * (4 * g + wave) + (tp_ >> 1)) + 8 * (tp_ & 1));
                        wv[4 * (c - 8) + 2 * j + tt] = mk8(lo, hi);
                    }
            } else {
#pragma unroll
                for (int j = 0; j < 2; ++j)
#pragma unroll
                    for (int kk = 0; kk < 2; ++kk)
#pragma unroll
                        for (int half = 0; half < 2; ++half) {
                            const unsigned char* tb = j ? t1 : t0;
                            const int e0 = 32 * kk + 16 * (G & 1) + 4 * (2 * half + (G >> 1)) + tq_;
                            v2u A = tr_read(tb + offB(e0, 4 * wave + (tp_ >> 1)) + 8 * (tp_ & 1));
                            v2u B = tr_read(tb + offB(e0, 4 * wave + 2 + (tp_ >> 1)) + 8 * (tp_ & 1));
                            const int dp = 8 * (c - 12) + 4 * j + wave;
                            *(v4u*)(lds + L_WO + ((dp * 2 + kk) * 2 + half) * 1024 + lane * 16) = (v4u){A.x, B.x, A.y, B.y};
                        }
            }
            asm volatile("s_waitcnt lgkmcnt(0)" ::: "memory");
            __builtin_amdgcn_s_barrier();
        }
    }
    const int sidx = (lane & 8) ? 0xEEEE : 0x4444;
    const int tq = l15 >> 2, tp = lane & 3;
    const unsigned char* memrd = lds + L_MEM + (8 * G + tq) * 16 + (tp & 1) * 8;
    const unsigned char* trd = lds + L_T + (8 * G + tq) * 32 + (tp & 1) * 8;
    const unsigned char* wp = lds + L_WO + wave * 16384 + lane * 16;
    const float sl2e = 0.08838834764831845f * 1.4426950408889634f;
    const bool exist0 = (G < 2);
    constexpr int PD = 4;
    const __amdgpu_buffer_rsrc_t rk = __builtin_amdgcn_make_buffer_rsrc(a.K, 0, S_ * HD * 2, 0x00020000);
    const __amdgpu_buffer_rsrc_t rv = __builtin_amdgcn_make_buffer_rsrc(a.VT, 0, S_ * HD * 2, 0x00020000);
    const __amdgpu_buffer_rsrc_t rkm = __builtin_amdgcn_make_buffer_rsrc(a.KM, 0, NSEG * 8 * HD * 2, 0x00020000);
    const __amdgpu_buffer_rsrc_t rvm = __builtin_amdgcn_make_buffer_rsrc(a.VMT, 0, NSEG * 8 * HD * 2, 0x00020000);

    wait_flag(a.flags + h * FLAG_STRIDE);
    wait_flag(a.flags + (32 + h) * FLAG_STRIDE);
    const unsigned lds0 = __builtin_amdgcn_readfirstlane(lds_addr(lds));
    const u16* ksrc0; const u16* ksrc1; const u16* vsrc;
    {
        const int p0 = tid, p1 = tid + 256;
        ksrc0 = a.K + (size_t)(p0 >> 4) * HD + h * DH + (((p0 & 15) ^ ((p0 >> 4) & 15)) * 8);
        ksrc1 = a.K + (size_t)(p1 >> 4) * HD + h * DH + (((p1 & 15) ^ ((p1 >> 4) & 15)) * 8);
        vsrc = a.VT + ((size_t)h * DH + 64 * g) * 32 + tid * 8;
    }
    auto load_kv = [&](int s) {
        glds16_sc1(ksrc0 + (size_t)s * 32 * HD, lds0 + L_KX + wave * 1024);
        glds16_sc1(ksrc1 + (size_t)s * 32 * HD, lds0 + L_KX + 4096 + wave * 1024);
        glds16_sc1(vsrc + (size_t)s * NH * DH * 32, lds0 + L_VX + wave * 1024);
    };
    v4u kx[2][4]; v2u vx[2];
    auto load_kx = [&](int s) {
#pragma unroll
        for (int kt = 0; kt < 2; ++kt)
#pragma unroll
            for (int ks = 0; ks < 4; ++ks) kx[kt][ks] = __builtin_amdgcn_raw_buffer_load_b128(rk, (unsigned)((s * 32 + kt * 16 + l15) * HD + h * DH + ks * 32 + 8 * G) * 2, 0, 16);
        const unsigned vo = (unsigned)(((s * NH + h) * DH + 64 * g + wave * 16 + l15) * 32 + 4 * G) * 2;
        vx[0] = __builtin_amdgcn_raw_buffer_load_b64(rv, vo, 0, 16); vx[1] = __builtin_amdgcn_raw_buffer_load_b64(rv, vo + 32, 0, 16);
    };
    load_kx(0);
    unsigned long long tpub = 0;
    DIAG_DECL
    for (int s = 0; s < NSEG; ++s) {
        const int sn = (s + 1 < NSEG) ? s + 1 : s;
        {
            v4u m0, m1;
            unsigned spins = 0;
            {
                unsigned sp2 = 0;
                while (__builtin_amdgcn_readfirstlane(__hip_atomic_load(a.flags + (SIG_BASE + 128 + 2 * b) * FLAG_STRIDE, __ATOMIC_RELAXED, __HIP_MEMORY_SCOPE_AGENT)) < (unsigned)s) {
                    if (++sp2 > SPIN_CAP) break;
                }
                asm volatile("" ::: "memory");
            }
            for (;;) {
                m0 = __builtin_amdgcn_raw_buffer_load_b128(rmem, s * MEMX_STEP + tid * 16, 0, 16);
                m1 = __builtin_amdgcn_raw_buffer_load_b128(rmem, s * MEMX_STEP + 4096 + tid * 16, 0, 16);
                *(v4u*)(lds + L_MEM + tid * 16) = m0;
                *(v4u*)(lds + L_MEM + 4096 + tid * 16) = m1;
                unsigned mx = umax3(umax3(m0.x, m0.y, m0.z), umax3(m0.w, m1.x, m1.y), max(m1.z, m1.w));
                asm volatile("" ::: "memory");
                if (__all(mx != 0xFFFFFFFFu)) break;
                if (++spins > SPIN_CAP) break;
            }
        }
        f32x4 acc0 = (f32x4){0.f, 0.f, 0.f, 0.f}, acc1 = acc0;
        asm volatile("" : "+v"(acc0), "+v"(acc1));
        STAMP(0);
        __syncthreads();
        XSIG(0);
        STAMP(1);
        v2u lo[16], hi[16];
#pragma unroll
        for (int t = 0; t < PD; ++t) { lo[t] = tr_read(memrd + t * 512); hi[t] = tr_read(memrd + t * 512 + 64); }
        SB0();
#pragma unroll
        for (int t = 0; t < 16; ++t) {
            if (t + PD < 16) { lo[t + PD] = tr_read(memrd + (t + PD) * 512); hi[t + PD] = tr_read(memrd + (t + PD) * 512 + 64); }
            acc0 = __builtin_amdgcn_smfmac_f32_16x16x64_bf16(mk8(lo[t], hi[t]), wqk[0][t], acc0, sidx, 0, 0);
            acc1 = __builtin_amdgcn_smfmac_f32_16x16x64_bf16(mk8(lo[t], hi[t]), wqk[1][t], acc1, sidx, 0, 0);
            SB0();
        }
        {
            v2u v0 = {pack2(acc0[0], acc0[1]), pack2(acc0[2], acc0[3])};
            v2u v1 = {pack2(acc1[0], acc1[1]), pack2(acc1[2], acc1[3])};
            *(v2u*)(lds + L_T + (wave * 32 + l15) * 32 + G * 8) = v0;
            *(v2u*)(lds + L_T + (wave * 32 + 16 + l15) * 32 + G * 8) = v1;
        }
        STAMP(2);
        __syncthreads();
        XSIG(1);
        STAMP(3);
        v2u q_lo[4], q_hi[4], k_lo[4], k_hi[4];
#pragma unroll
        for (int ks = 0; ks < 4; ++ks) {
            q_lo[ks] = tr_read(trd + ks * 1024); q_hi[ks] = tr_read(trd + ks * 1024 + 128);
            k_lo[ks] = tr_read(trd + ks * 1024 + 16); k_hi[ks] = tr_read(trd + ks * 1024 + 128 + 16);
        }
        f32x4 accv = (f32x4){0.f, 0.f, 0.f, 0.f};
#pragma unroll
        for (int t = 0; t < 4; ++t) accv = mfma16(mk8(lo[t], hi[t]), wv[t], accv);
        SB0();
        f32x4 st[3];
#pragma unroll
        for (int i = 0; i < 3; ++i) st[i] = (f32x4){0.f, 0.f, 0.f, 0.f};
#pragma unroll
        for (int ks = 0; ks < 4; ++ks) {
            bf16x8 qf = mk8(q_lo[ks], q_hi[ks]);
            st[1] = mfma16(as8(kx[0][ks]), qf, st[1]);
            st[2] = mfma16(as8(kx[1][ks]), qf, st[2]);
            st[0] = mfma16(mk8(k_lo[ks], k_hi[ks]), qf, st[0]);
        }
        XSIG(2);
        v4u wo[4][2][2];
#pragma unroll
        for (int i = 0; i < 4; ++i) { wo[i][0][0] = *(const v4u*)(wp + i * 4096); wo[i][0][1] = *(const v4u*)(wp + i * 4096 + 1024); }
        SB0();
#pragma unroll
        for (int t = 4; t < 16; ++t) accv = mfma16(mk8(lo[t], hi[t]), wv[t], accv);
        float m = fmaxf(fmaxf(fmaxf(st[1][0], st[1][1]), fmaxf(st[1][2], st[1][3])), fmaxf(fmaxf(st[2][0], st[2][1]), fmaxf(st[2][2], st[2][3])));
        float m0 = fmaxf(fmaxf(st[0][0], st[0][1]), fmaxf(st[0][2], st[0][3]));
        m = exist0 ? fmaxf(m, m0) : m;
        m = xrow16_max(m);
        const float nm = -m * sl2e;
        float v[3][4];
#pragma unroll
        for (int i = 0; i < 3; ++i)
#pragma unroll
            for (int r = 0; r < 4; ++r) v[i][r] = __builtin_amdgcn_exp2f(__builtin_fmaf(st[i][r], sl2e, nm));
#pragma unroll
        for (int r = 0; r < 4; ++r) v[0][r] = exist0 ? v[0][r] : 0.f;
        v4u p0 = {pack2(v[0][0], v[0][1]), pack2(v[0][2], v[0][3]), pack2(v[1][0], v[1][1]), pack2(v[1][2], v[1][3])};
        v4u p1 = {pack2(v[2][0], v[2][1]), pack2(v[2][2], v[2][3]), 0u, 0u};
        XSIG(5);
        v2u vm = {pack2(accv[0], accv[1]), pack2(accv[2], accv[3])};
        f32x4 o = mfma16(mk8(vx[1], (v2u){0u, 0u}), as8(p1), (f32x4){0.f, 0.f, 0.f, 0.f});
        o = mfma16(mk8(vm, vx[0]), as8(p0), o);
        float sum = ((v[0][0] + v[0][1]) + (v[0][2] + v[0][3])) + ((v[1][0] + v[1][1]) + (v[1][2] + v[1][3])) + ((v[2][0] + v[2][1]) + (v[2][2] + v[2][3]));
        sum = xrow16_sum(sum);
        const float inv = __builtin_amdgcn_rcpf(sum);
        *(v2u*)(lds + L_VM + wave * 512 + lane * 8) = (v2u){pack2(o[0] * inv, o[1] * inv), pack2(o[2] * inv, o[3] * inv)};
        f32x4 d[4];
#pragma unroll
        for (int i = 0; i < 4; ++i) d[i] = (f32x4){0.f, 0.f, 0.f, 0.f};
        asm volatile("" : "+v"(d[0]), "+v"(d[1]), "+v"(d[2]), "+v"(d[3]));
        XSIG(3);
        STAMP(4);
        __syncthreads();
        XSIG(4);
        STAMP(5);
        v2u ob[4];
#pragma unroll
        for (int et = 0; et < 4; ++et) ob[et] = *(const v2u*)(lds + L_VM + et * 512 + lane * 8);
#pragma unroll
        for (int i = 0; i < 4; ++i) { wo[i][1][0] = *(const v4u*)(wp + i * 4096 + 2048); wo[i][1][1] = *(const v4u*)(wp + i * 4096 + 3072); }
        SB0();
        const bf16x8 oa0 = mk8(ob[0], ob[1]), oa1 = mk8(ob[2], ob[3]);
#pragma unroll
        for (int kk = 0; kk < 2; ++kk)
#pragma unroll
            for (int i = 0; i < 4; ++i) {
                bf16x16 wf;
                bf16x8 w0b = as8(wo[i][kk][0]), w1b = as8(wo[i][kk][1]);
#pragma unroll
                for (int j = 0; j < 8; ++j) { wf[j] = w0b[j]; wf[8 + j] = w1b[j]; }
                d[i] = __builtin_amdgcn_smfmac_f32_16x16x64_bf16(kk == 0 ? oa0 : oa1, wf, d[i], sidx, 0, 0);
            }
#pragma unroll
        for (int i = 0; i < 4; ++i) {
            const int dcol = 32 * (wave * 4 + i) + 16 * (G >> 1) + l15;
            v2u pv = {nosent(pack2(d[i][0], d[i][1])), nosent(pack2(d[i][2], d[i][3]))};
            __builtin_amdgcn_raw_buffer_store_b64(pv, rpart, (s & 1) * PART_BUF + (dcol >> 3) * 8192 + b * 128 + (dcol & 7) * 16 + (G & 1) * 8, 0, 16);
        }
        STAMP(6);
        if (G < 2) {
            v2u vs = {nosent(vm.x), nosent(vm.y)};
            __builtin_amdgcn_raw_buffer_store_b64(vs, rvm, (unsigned)(((s * NH + h) * DH + 64 * g + wave * 16 + l15) * 8 + 4 * G) * 2, 0, 16);
        }
        if (g == 0 && l15 < 8) {
            v2u kl = wave == 0 ? k_lo[0] : (wave == 1 ? k_lo[1] : (wave == 2 ? k_lo[2] : k_lo[3]));
            v2u kh = wave == 0 ? k_hi[0] : (wave == 1 ? k_hi[1] : (wave == 2 ? k_hi[2] : k_hi[3]));
            v4u kv = {nosent(kl.x), nosent(kl.y), nosent(kh.x), nosent(kh.y)};
            __builtin_amdgcn_raw_buffer_store_b128(kv, rkm, (unsigned)((s * 8 + l15) * HD + h * DH + wave * 32 + 8 * G) * 2, 0, 16);
        }
        SB0();
        load_kx(sn);
        if ((s & 3) == 2 && s + 2 < NSEG) {
            wait_flag(a.flags + (((s + 2) >> 2) * 96 + h) * FLAG_STRIDE);
            wait_flag(a.flags + (((s + 2) >> 2) * 96 + 32 + h) * FLAG_STRIDE);
        }
    }
    DIAG_END;
}

extern "C" void kernel_launch(void* const* d_in, const int* in_sizes, int n_in, void* d_out, int out_size, void* d_ws, size_t ws_size,
                              hipStream_t stream) {
    const u16* x = (const u16*)d_in[0];
    const u16* mem0 = (const u16*)d_in[1];
    const u16* Wq = (const u16*)d_in[2];
    const u16* Wk = (const u16*)d_in[3];
    const u16* Wv = (const u16*)d_in[4];
    const u16* Wo = (const u16*)d_in[5];
    const u16* Wg = (const u16*)d_in[8];
    const u16* Wu = (const u16*)d_in[9];
    const u16* Wd = (const u16*)d_in[10];
    u16* out = (u16*)d_out;

    unsigned char* ws = (unsigned char*)d_ws;
    size_t off = 0;
    auto alloc = [&](size_t bytes) { unsigned char* p = ws + off; off += (bytes + 255) & ~(size_t)255; return p; };
    constexpr size_t KM_BYTES = (size_t)NSEG * 8 * HD * 2;
    constexpr size_t SENT_BYTES = (size_t)2 * PART_BUF + (size_t)(NSEG + 1) * MEMX_STEP + 2 * KM_BYTES;
    unsigned* flags = (unsigned*)alloc((16 * 96 + 64 + 256) * FLAG_STRIDE * 4);
    unsigned char* xchg = alloc(SENT_BYTES);
    unsigned char* part = xchg;
    unsigned char* memx = xchg + 2 * PART_BUF;
    u16* KM = (u16*)(memx + (size_t)(NSEG + 1) * MEMX_STEP);
    u16* VMT = (u16*)((unsigned char*)KM + KM_BYTES);
    u16* xn = (u16*)alloc((size_t)S_ * DM * 2);
    u16* Q = (u16*)alloc((size_t)S_ * HD * 2);
    u16* K = (u16*)alloc((size_t)S_ * HD * 2);
    u16* VT = (u16*)alloc((size_t)S_ * HD * 2);
    u16* O = (u16*)alloc((size_t)S_ * HD * 2);
    u16* hb = (u16*)alloc((size_t)S_ * DM * 2);
    u16* hn = (u16*)alloc((size_t)S_ * DM * 2);
    u16* act = (u16*)alloc((size_t)S_ * FF * 2);
    float* Pk = (float*)alloc((size_t)16 * S_ * DM * 4);

    k_prep<<<S_ / 4, 256, 0, stream>>>(x, xn, mem0, (u16*)memx, (v4u*)xchg, (int)(SENT_BYTES / 16), (int)(2 * PART_BUF / 16), (v4u*)flags, (16 * 96 + 64 + 256) * FLAG_STRIDE / 4);
    {
        static bool attr_set = false;
        if (!attr_set) { (void)hipFuncSetAttribute((const void*)k_rec, hipFuncAttributeMaxDynamicSharedMemorySize, L_TOTAL); attr_set = true; }
        RecArgs r{Wo, K, VT, Q, O, KM, VMT, memx, part, xn, Wq, Wk, Wv, flags, x, Wg, Wu, Wd, hb, hn, act, out};
        k_rec<<<2 * NX + NWORK, 256, L_TOTAL, stream>>>(r);
    }
    {
        constexpr int ROW0 = RB_IN * 128, MR = S_ - ROW0;
        constexpr int ROWW = RB_W * 128, MW = S_ - ROWW, ROWF = RB_F1 * 128, MF = S_ - ROWF;
        constexpr int NZ = (MW <= 256) ? 16 : 8;
        static bool attr2 = false;
        if (!attr2) {
            (void)hipFuncSetAttribute((const void*)k_gemm2<1, 128, 1, 5>, hipFuncAttributeMaxDynamicSharedMemorySize, 5 * (128 * 128 + 8192));
            (void)hipFuncSetAttribute((const void*)k_gemm2<2, 64, 2, 4>, hipFuncAttributeMaxDynamicSharedMemorySize, 4 * (64 * 128 + 16384));
            (void)hipFuncSetAttribute((const void*)k_gemm2<3, 32, 1, 8>, hipFuncAttributeMaxDynamicSharedMemorySize, 8 * (32 * 128 + 8192));
            attr2 = true;
        }
        GemmArgs g1{O, Wo, nullptr, nullptr, nullptr, (u16*)Pk, nullptr, nullptr, HD, DM, HD / NZ, (DM / 64) | (NZ << 16)};
        k_gemm2<1, 128, 1, 5><<<(DM / 64) * (MW / 128) * NZ, 256, 5 * (128 * 128 + 8192), stream>>>(g1, ROWW);
        k_res_ln<NZ><<<MW / 4, 256, 0, stream>>>(Pk, x, hb, hn, ROWW);
        GemmArgs g2{hn, Wg, Wu, nullptr, nullptr, act, nullptr, nullptr, DM, FF, DM, (FF / 64) | (1 << 16)};
        k_gemm2<2, 64, 2, 4><<<(FF / 64) * (MF / 64), 256, 4 * (64 * 128 + 16384), stream>>>(g2, ROWF);
        GemmArgs g3{act, Wd, nullptr, nullptr, hb, out, nullptr, nullptr, FF, DM, FF, (DM / 64) | (1 << 16)};
        k_gemm2<3, 32, 1, 8><<<(DM / 64) * (MR / 32), 256, 8 * (32 * 128 + 8192), stream>>>(g3, ROW0);
    }
}
```

```cpp
#include <hip/hip_runtime.h>
#include <hip/hip_bf16.h>

typedef unsigned short u16;
typedef __bf16 bf16x8 __attribute__((ext_vector_type(8)));
typedef __bf16 bf16x16 __attribute__((ext_vector_type(16)));
typedef float f32x4 __attribute__((ext_vector_type(4)));
typedef unsigned v4u __attribute__((ext_vector_type(4)));
typedef unsigned v2u __attribute__((ext_vector_type(2)));
typedef short v4s __attribute__((ext_vector_type(4)));
#define LAS __attribute__((address_space(3)))

constexpr int S_ = 2048, DM = 512, NH = 32, DH = 128, HD = NH * DH, MEMN = 8, SEG = 32, NSEG = 64, FF = 1792;

__device__ __forceinline__ float bf2f(u16 v) { return __uint_as_float(((unsigned)v) << 16); }
__device__ __forceinline__ u16 f2bf(float f) { return __builtin_bit_cast(u16, (__bf16)f); }
__device__ __forceinline__ float bfr(float f) { return bf2f(f2bf(f)); }
typedef float f32x2 __attribute__((ext_vector_type(2)));
typedef __bf16 bf16x2 __attribute__((ext_vector_type(2)));
__device__ __forceinline__ unsigned pack2(float a, float b) { f32x2 t = {a, b}; return __builtin_bit_cast(unsigned, __builtin_convertvector(t, bf16x2)); }
__device__ __forceinline__ float lo16f(unsigned w) { return __uint_as_float(w << 16); }
__device__ __forceinline__ float hi16f(unsigned w) { return __uint_as_float(w & 0xFFFF0000u); }
template <int CTRL> __device__ __forceinline__ float dppf(float x) {
    return __builtin_bit_cast(float, __builtin_amdgcn_mov_dpp(__builtin_bit_cast(int, x), CTRL, 0xf, 0xf, true));
}
__device__ __forceinline__ float xrow16_max(float x) {
    auto s = __builtin_amdgcn_permlane16_swap(__float_as_uint(x), __float_as_uint(x), false, false);
    x = fmaxf(__uint_as_float(s[0]), __uint_as_float(s[1]));
    auto t = __builtin_amdgcn_permlane32_swap(__float_as_uint(x), __float_as_uint(x), false, false);
    return fmaxf(__uint_as_float(t[0]), __uint_as_float(t[1]));
}
__device__ __forceinline__ float xrow16_sum(float x) {
    auto s = __builtin_amdgcn_permlane16_swap(__float_as_uint(x), __float_as_uint(x), false, false);
    x = __uint_as_float(s[0]) + __uint_as_float(s[1]);
    auto t = __builtin_amdgcn_permlane32_swap(__float_as_uint(x), __float_as_uint(x), false, false);
    return __uint_as_float(t[0]) + __uint_as_float(t[1]);
}
__device__ __forceinline__ unsigned umax3(unsigned a, unsigned b, unsigned c) { return max(max(a, b), c); }
__device__ __forceinline__ unsigned umax4(v4u v) { return max(umax3(v.x, v.y, v.z), v.w); }
#define SB0() __builtin_amdgcn_sched_barrier(0)
__device__ __forceinline__ void glds16(const void* gsrc, unsigned lds_dst) {
    unsigned keep;
    asm volatile("s_mov_b32 %0, m0\n\ts_mov_b32 m0, %2\n\ts_nop 0\n\tglobal_load_lds_dwordx4 %1, off\n\ts_mov_b32 m0, %0" : "=&s"(keep) : "v"(gsrc), "s"(lds_dst) : "memory");
}
__device__ __forceinline__ unsigned lds_addr(const void* p) { return (unsigned)(size_t)(LAS const unsigned char*)p; }
__device__ __forceinline__ unsigned nosent(unsigned w) { return min(w, 0xFFFFFFFEu); }
__device__ __forceinline__ v2u tr_read(const void* p) {
    return __builtin_bit_cast(v2u, __builtin_amdgcn_ds_read_tr16_b64_v4i16((LAS v4s*)p));
}
__device__ __forceinline__ bf16x8 mk8(v2u a, v2u b) { v4u t = {a.x, a.y, b.x, b.y}; return __builtin_bit_cast(bf16x8, t); }
__device__ __forceinline__ bf16x8 as8(v4u t) { return __builtin_bit_cast(bf16x8, t); }
__device__ __forceinline__ f32x4 mfma16(bf16x8 a, bf16x8 b, f32x4 c) { return __builtin_amdgcn_mfma_f32_16x16x32_bf16(a, b, c, 0, 0, 0); }

__device__ __forceinline__ float wave_sum(float s) {
    s += dppf<0xB1>(s); s += dppf<0x4E>(s); s += dppf<0x141>(s); s += dppf<0x128>(s);
    return xrow16_sum(s);
}
__device__ __forceinline__ v4u layernorm_vals(v4u raw) {
    float x[8];
#pragma unroll
    for (int i = 0; i < 4; ++i) { x[2 * i] = lo16f(raw[i]); x[2 * i + 1] = hi16f(raw[i]); }
    float s = ((x[0] + x[1]) + (x[2] + x[3])) + ((x[4] + x[5]) + (x[6] + x[7]));
    const float mean = wave_sum(s) * (1.0f / DM);
    float q = 0.f;
#pragma unroll
    for (int i = 0; i < 8; ++i) { float d = x[i] - mean; q += d * d; }
    q = wave_sum(q);
    const float mb = bfr(mean);
    const float vb = bfr(q * (1.0f / DM));
    const float den = bfr(sqrtf(bfr(vb + 1e-5f)));
    v4u outv;
#pragma unroll
    for (int i = 0; i < 4; ++i) {
        float a = bfr(x[2 * i] - mb) / den, b = bfr(x[2 * i + 1] - mb) / den;
        outv[i] = pack2(a, b);
    }
    return outv;
}
__device__ __forceinline__ void layernorm_row(const u16* __restrict__ src, u16* __restrict__ dst, int lane) {
    *(v4u*)(dst + lane * 8) = layernorm_vals(*(const v4u*)(src + lane * 8));
}

__global__ void __launch_bounds__(256) k_prep(const u16* __restrict__ x, u16* __restrict__ xn, const u16* __restrict__ mem0, u16* __restrict__ memx,
                                              v4u* __restrict__ sent, int n_sent16, int memx_off16, v4u* __restrict__ flags, int n_flag16) {
    const int wave = threadIdx.x >> 6, lane = threadIdx.x & 63;
    const int row = blockIdx.x * 4 + wave;
    if (row < S_) layernorm_row(x + (size_t)row * DM, xn + (size_t)row * DM, lane);
    const v4u ones = {0xFFFFFFFFu, 0xFFFFFFFFu, 0xFFFFFFFFu, 0xFFFFFFFFu};
    const int gt = blockIdx.x * 256 + threadIdx.x, nt = gridDim.x * 256;
    for (int i = gt; i < n_sent16; i += nt)
        if (i < memx_off16 || i >= memx_off16 + 512) sent[i] = ones;
    for (int i = gt; i < n_flag16; i += nt) flags[i] = (v4u){0u, 0u, 0u, 0u};
    if (blockIdx.x == 0) {
        for (int i = threadIdx.x; i < MEMN * DM; i += 256) {
            int k = i >> 3, r = i & 7;
            u16 v = mem0[r * DM + k];
            if (v == 0xFFFFu) v = 0x7FFFu;
            memx[i] = v;
        }
    }
}

struct GemmArgs {
    const u16* A; const u16* B0; const u16* B1; const u16* B2; const u16* res;
    u16* C0; u16* C1; u16* C2;
    int lda, ldb, K, pad;
};

__device__ __forceinline__ unsigned offB(unsigned row, unsigned ch) { return 256u * row + 16u * (ch ^ (((row & 3) << 2) | ((row >> 2) & 3))); }
__device__ __forceinline__ unsigned offA(unsigned row, unsigned ch) { return 128u * row + 16u * (ch ^ (row & 7)); }

__device__ __forceinline__ unsigned fB(unsigned row) { return (((row >> 1) & 1) | (((row >> 3) & 1) << 1)) << 1; }
template <int MODE, int BM, int NB, int NST, bool SC1OUT>
__device__ __forceinline__ void gemm_body(const GemmArgs& g, unsigned char* sm, int bm, int bn, int bz) {
    constexpr int MT = BM / 32;
    constexpr int ABYTES = BM * 128, STAGE = ABYTES + NB * 8192;
    constexpr int LA = BM / 32, LPS = LA + 2 * NB;
    const int tid = threadIdx.x, lane = tid & 63;
    const int wave = __builtin_amdgcn_readfirstlane(tid >> 6);
    const int wm = wave >> 1, wn = wave & 1;
    const int kbase = bz * g.K;
    const u16* Asrc[LA];
    const u16* Bsrc[NB][2];
#pragma unroll
    for (int i = 0; i < LA; ++i) { int q = tid + 256 * i; int row = q >> 3, ch = (q & 7) ^ (row & 7); Asrc[i] = g.A + (size_t)(bm + row) * g.lda + kbase + ch * 8; }
#pragma unroll
    for (int n = 0; n < NB; ++n)
#pragma unroll
        for (int i = 0; i < 2; ++i) { int q = tid + 256 * i; int row = q >> 3, ch = (q & 7) ^ fB(row); Bsrc[n][i] = (n == 0 ? g.B0 : g.B1) + (size_t)(kbase + row) * g.ldb + bn + ch * 8; }
    const unsigned sm_base = __builtin_amdgcn_readfirstlane(lds_addr(sm)) + wave * 1024;
    auto stage = [&](int kt) {
        const unsigned base = sm_base + (kt % NST) * STAGE;
#pragma unroll
        for (int i = 0; i < LA; ++i) glds16(Asrc[i] + kt * 64, base + i * 4096);
#pragma unroll
        for (int n = 0; n < NB; ++n)
#pragma unroll
            for (int i = 0; i < 2; ++i) glds16(Bsrc[n][i] + (size_t)kt * 64 * g.ldb, base + ABYTES + n * 8192 + i * 4096);
    };
    f32x4 acc[NB][MT][2];
#pragma unroll
    for (int n = 0; n < NB; ++n)
#pragma unroll
        for (int i = 0; i < MT; ++i) { acc[n][i][0] = (f32x4){0.f, 0.f, 0.f, 0.f}; acc[n][i][1] = acc[n][i][0]; }
    const int nk = g.K / 64;
#pragma unroll
    for (int t = 0; t < NST - 1; ++t) stage(t);
    const int G = lane >> 4, l15 = lane & 15, tq = l15 >> 2, tp = lane & 3;
    for (int kt = 0; kt < nk; ++kt) {
        if (kt + NST - 1 <= nk) asm volatile("s_waitcnt vmcnt(%0)" :: "i"((NST - 2) * LPS) : "memory");
        else asm volatile("s_waitcnt vmcnt(0)" ::: "memory");
        asm volatile("s_waitcnt lgkmcnt(0)" ::: "memory");
        __builtin_amdgcn_s_barrier();
        if (kt + NST - 1 < nk) stage(kt + NST - 1);
        const unsigned char* sA = sm + (kt % NST) * STAGE;
        const unsigned char* sB = sA + ABYTES;
#pragma unroll
        for (int ks = 0; ks < 2; ++ks) {
            bf16x8 af[MT], bq[NB][2];
#pragma unroll
            for (int i = 0; i < MT; ++i) af[i] = as8(*(const v4u*)(sA + offA(wm * (BM / 2) + i * 16 + l15, ks * 4 + G)));
#pragma unroll
            for (int n = 0; n < NB; ++n)
#pragma unroll
                for (int j = 0; j < 2; ++j) {
                    const unsigned r0 = 32 * ks + 8 * G + tq, r1 = r0 + 4, ch = (wn * 2 + j) * 2 + (tp >> 1);
                    v2u lo = tr_read(sB + n * 8192 + r0 * 128 + ((ch ^ fB(r0)) * 16) + 8 * (tp & 1));
                    v2u hi = tr_read(sB + n * 8192 + r1 * 128 + ((ch ^ fB(r1)) * 16) + 8 * (tp & 1));
                    bq[n][j] = mk8(lo, hi);
                }
#pragma unroll
            for (int n = 0; n < NB; ++n)
#pragma unroll
                for (int i = 0; i < MT; ++i)
#pragma unroll
                    for (int j = 0; j < 2; ++j) acc[n][i][j] = mfma16(bq[n][j], af[i], acc[n][i][j]);
        }
    }
#pragma unroll
    for (int i = 0; i < MT; ++i)
#pragma unroll
        for (int j = 0; j < 2; ++j) {
            const int row = bm + wm * (BM / 2) + i * 16 + l15;
            const int col = bn + (wn * 2 + j) * 16 + 4 * G;
            if (MODE == 1) {
                float* P = (float*)g.C0 + ((size_t)bz * S_ + row) * DM + col;
                *(f32x4*)P = acc[0][i][j];
            } else if (MODE == 2) {
                unsigned g0 = pack2(acc[0][i][j][0], acc[0][i][j][1]), g1 = pack2(acc[0][i][j][2], acc[0][i][j][3]);
                unsigned u0 = pack2(acc[NB - 1][i][j][0], acc[NB - 1][i][j][1]), u1 = pack2(acc[NB - 1][i][j][2], acc[NB - 1][i][j][3]);
                v2u v = {pack2(lo16f(g0) * lo16f(u0), hi16f(g0) * hi16f(u0)), pack2(lo16f(g1) * lo16f(u1), hi16f(g1) * hi16f(u1))};
                if (SC1OUT) __hip_atomic_store((unsigned long long*)(g.C0 + (size_t)row * FF + col), __builtin_bit_cast(unsigned long long, v), __ATOMIC_RELAXED, __HIP_MEMORY_SCOPE_AGENT);
                else *(v2u*)(g.C0 + (size_t)row * FF + col) = v;
            } else {
                v2u r = *(const v2u*)(g.res + (size_t)row * DM + col);
                unsigned a0 = pack2(acc[0][i][j][0], acc[0][i][j][1]), a1 = pack2(acc[0][i][j][2], acc[0][i][j][3]);
                v2u v = {pack2(lo16f(r.x) + lo16f(a0), hi16f(r.x) + hi16f(a0)), pack2(lo16f(r.y) + lo16f(a1), hi16f(r.y) + hi16f(a1))};
                if (SC1OUT) __hip_atomic_store((unsigned long long*)(g.C0 + (size_t)row * DM + col), __builtin_bit_cast(unsigned long long, v), __ATOMIC_RELAXED, __HIP_MEMORY_SCOPE_AGENT);
                else *(v2u*)(g.C0 + (size_t)row * DM + col) = v;
            }
        }
}

template <int MODE, int BM, int NB, int NST>
__global__ void __launch_bounds__(256) k_gemm2(GemmArgs g, int row0) {
    extern __shared__ __attribute__((aligned(16))) unsigned char sm[];
    const int ncol = g.pad & 0xFFFF, nz = g.pad >> 16;
    const int per_xcd = gridDim.x >> 3;
    const int lin = (blockIdx.x & 7) * per_xcd + (blockIdx.x >> 3);
    const int panel = lin / ncol;
    gemm_body<MODE, BM, NB, NST, false>(g, sm, row0 + (panel / nz) * BM, (lin % ncol) * 64, panel % nz);
}

template <int NZ>
__global__ void __launch_bounds__(256) k_res_ln(const float* __restrict__ P, const u16* __restrict__ x, u16* __restrict__ hb, u16* __restrict__ hn, int row0) {
    const int wave = threadIdx.x >> 6, lane = threadIdx.x & 63;
    const int row = row0 + blockIdx.x * 4 + wave;
    f32x4 s0 = *(const f32x4*)(P + (size_t)row * DM + lane * 8), s1 = *(const f32x4*)(P + (size_t)row * DM + lane * 8 + 4);
#pragma unroll
    for (int z = 1; z < NZ; ++z) { s0 += *(const f32x4*)(P + ((size_t)z * S_ + row) * DM + lane * 8); s1 += *(const f32x4*)(P + ((size_t)z * S_ + row) * DM + lane * 8 + 4); }
    v4u raw = *(const v4u*)(x + (size_t)row * DM + lane * 8);
    unsigned a0 = pack2(s0[0], s0[1]), a1 = pack2(s0[2], s0[3]), a2 = pack2(s1[0], s1[1]), a3 = pack2(s1[2], s1[3]);
    v4u hv = {pack2(lo16f(raw.x) + lo16f(a0), hi16f(raw.x) + hi16f(a0)), pack2(lo16f(raw.y) + lo16f(a1), hi16f(raw.y) + hi16f(a1)),
              pack2(lo16f(raw.z) + lo16f(a2), hi16f(raw.z) + hi16f(a2)), pack2(lo16f(raw.w) + lo16f(a3), hi16f(raw.w) + hi16f(a3))};
    *(v4u*)(hb + (size_t)row * DM + lane * 8) = hv;
    *(v4u*)(hn + (size_t)row * DM + lane * 8) = layernorm_vals(hv);
}

constexpr int NX = 64;
constexpr int MEMX_STEP = 8192;
constexpr int PART_BUF = 64 * 64 * 128;
constexpr unsigned SPIN_CAP = 1u << 22;

struct RecArgs {
    const u16* Wo;
    u16* K; u16* VT; u16* Q; u16* O;
    u16* KM; u16* VMT;
    unsigned char* memx; unsigned char* part;
    const u16* xn; const u16* Wq; const u16* Wk; const u16* Wv;
    unsigned* flags;
    const u16* x; const u16* Wg; const u16* Wu; const u16* Wd;
    u16* hb; u16* hn; u16* act; u16* out;
};
constexpr int RB_W = 13, RB_F1 = 13;
constexpr int RB_IN = 12;
constexpr int SIG_BASE = 16 * 96 + 64;
constexpr int CNT_BASE = 16 * 96;
constexpr int NWORK = 128;
constexpr int FLAG_STRIDE = 16;

__device__ __forceinline__ void wait_flag(const unsigned* f) {
    unsigned spins = 0;
    while (__builtin_amdgcn_readfirstlane(__hip_atomic_load(f, __ATOMIC_RELAXED, __HIP_MEMORY_SCOPE_AGENT)) == 0u) {
        __builtin_amdgcn_s_sleep(2);
        if (++spins > SPIN_CAP) break;
    }
    asm volatile("" ::: "memory");
}

__device__ __forceinline__ void wait_count_acquire(const unsigned* c, unsigned target) {
    if (threadIdx.x < 64) {
        unsigned spins = 0;
        while (__builtin_amdgcn_readfirstlane(__hip_atomic_load(c, __ATOMIC_RELAXED, __HIP_MEMORY_SCOPE_AGENT)) < target) {
            __builtin_amdgcn_s_sleep(32);
            if (++spins > SPIN_CAP) break;
        }
        __builtin_amdgcn_fence(__ATOMIC_ACQUIRE, "agent");
        asm volatile("s_waitcnt vmcnt(0)" ::: "memory");
    }
    __syncthreads();
}
__device__ __forceinline__ void signal_count(unsigned* c) {
    asm volatile("s_waitcnt vmcnt(0)" ::: "memory");
    __syncthreads();
    if (threadIdx.x == 0) __hip_atomic_fetch_add(c, 1u, __ATOMIC_RELAXED, __HIP_MEMORY_SCOPE_AGENT);
}

template <bool ISV>
__device__ __forceinline__ void proj_col(const RecArgs& a, unsigned char* lds, int c) {
    constexpr int NST = 8;
    const int tid = threadIdx.x, lane = tid & 63;
    const int wave = __builtin_amdgcn_readfirstlane(tid >> 6);
    const int wm = wave >> 1, wn = wave & 1;
    const int mat = c >> 5, head = c & 31;
    const int G = lane >> 4, l15 = lane & 15, tq = l15 >> 2, tp = lane & 3;
    const unsigned sbase = __builtin_amdgcn_readfirstlane(lds_addr(lds)) + wave * 1024;
    bf16x8 bw[4][16];
    {
        const u16* Bp = (mat == 0 ? a.Wk : (mat == 1 ? a.Wv : a.Wq)) + head * DH;
#pragma unroll
        for (int kt = 0; kt < 8; ++kt)
#pragma unroll
            for (int i = 0; i < 4; ++i) {
                const int q = tid + 256 * i, r = q >> 4;
                glds16(Bp + (size_t)(kt * 64 + r) * HD + (((q & 15) ^ (((r & 3) << 2) | ((r >> 2) & 3))) * 8), sbase + kt * 16384 + i * 4096);
            }
        asm volatile("s_waitcnt vmcnt(0)" ::: "memory");
        __builtin_amdgcn_s_barrier();
#pragma unroll
        for (int t = 0; t < 16; ++t)
#pragma unroll
            for (int j = 0; j < 4; ++j) {
                const unsigned char* sB = lds + (t >> 1) * 16384;
                const int cc = wn * 4 + j, ks = t & 1;
                v2u lo = tr_read(sB + offB(32 * ks + 8 * G + tq, 2 * cc + (tp >> 1)) + 8 * (tp & 1));
                v2u hi = tr_read(sB + offB(32 * ks + 8 * G + 4 + tq, 2 * cc + (tp >> 1)) + 8 * (tp & 1));
                bw[j][t] = mk8(lo, hi);
            }
        asm volatile("s_waitcnt lgkmcnt(0)" ::: "memory");
        __builtin_amdgcn_s_barrier();
    }
    const u16* asrc[4];
#pragma unroll
    for (int i = 0; i < 4; ++i) { const int q = tid + 256 * i; asrc[i] = a.xn + (size_t)(q >> 3) * DM + (((q & 7) ^ ((q >> 3) & 7)) * 8); }
    auto stage = [&](int n) {
        const unsigned d = sbase + (n & (NST - 1)) * 16384;
        const size_t off = (size_t)(n >> 3) * 128 * DM + (n & 7) * 64;
#pragma unroll
        for (int i = 0; i < 4; ++i) glds16(asrc[i] + off, d + i * 4096);
    };
#pragma unroll
    for (int n = 0; n < NST - 1; ++n) stage(n);
    unsigned char* img = lds + 131072;
#pragma unroll 1
    for (int rb = 0; rb < 16; ++rb) {
        f32x4 acc[4][4];
#pragma unroll
        for (int i = 0; i < 4; ++i)
#pragma unroll
            for (int j = 0; j < 4; ++j) acc[i][j] = (f32x4){0.f, 0.f, 0.f, 0.f};
#pragma unroll
        for (int kt = 0; kt < 8; ++kt) {
            const int n = rb * 8 + kt;
            if (n + NST - 1 > 128) asm volatile("s_waitcnt vmcnt(0)" ::: "memory");
            else if (kt < 7 && rb > 0) asm volatile("s_waitcnt vmcnt(32)" ::: "memory");
            else asm volatile("s_waitcnt vmcnt(24)" ::: "memory");
            asm volatile("s_waitcnt lgkmcnt(0)" ::: "memory");
            __builtin_amdgcn_s_barrier();
            if (kt == 7 && rb >= 2 && tid == 0) __hip_atomic_store(a.flags + ((rb - 1) * 96 + c) * FLAG_STRIDE, 1u, __ATOMIC_RELAXED, __HIP_MEMORY_SCOPE_AGENT);
            if (n + NST - 1 < 128) stage(n + NST - 1);
            const unsigned char* sA = lds + kt * 16384;
#pragma unroll
            for (int ks = 0; ks < 2; ++ks) {
                bf16x8 af[4];
#pragma unroll
                for (int i = 0; i < 4; ++i) af[i] = as8(*(const v4u*)(sA + offA(wm * 64 + i * 16 + l15, ks * 4 + G)));
#pragma unroll
                for (int i = 0; i < 4; ++i)
#pragma unroll
                    for (int j = 0; j < 4; ++j) acc[i][j] = ISV ? mfma16(af[i], bw[j][kt * 2 + ks], acc[i][j]) : mfma16(bw[j][kt * 2 + ks], af[i], acc[i][j]);
            }
        }
#pragma unroll
        for (int i = 0; i < 4; ++i)
#pragma unroll
            for (int j = 0; j < 4; ++j) {
                v2u v = {pack2(acc[i][j][0], acc[i][j][1]), pack2(acc[i][j][2], acc[i][j][3])};
                if (ISV) {
                    const int e = (wn * 4 + j) * 16 + l15, row0 = wm * 64 + i * 16 + 4 * G;
                    const int line = (row0 >> 5) * 128 + e;
                    *(v2u*)(img + line * 64 + ((((row0 & 31) >> 2) ^ (e & 7)) * 8)) = v;
                } else {
                    const int row = wm * 64 + i * 16 + l15, col0 = (wn * 4 + j) * 16 + 4 * G;
                    *(v2u*)(img + row * 256 + (((col0 >> 3) ^ (row & 15)) * 16) + ((col0 & 4) * 2)) = v;
                }
            }
        asm volatile("s_waitcnt lgkmcnt(0)" ::: "memory");
        __builtin_amdgcn_s_barrier();
        if (ISV) {
            const __amdgpu_buffer_rsrc_t r = __builtin_amdgcn_make_buffer_rsrc(a.VT, 0, S_ * HD * 2, 0x00020000);
#pragma unroll
            for (int i = 0; i < 8; ++i) {
                const int q = tid + 256 * i;
                const int line = q >> 2, cq = q & 3, e = line & 127;
                v2u x0 = *(const v2u*)(img + line * 64 + (((2 * cq) ^ (e & 7)) * 8));
                v2u x1 = *(const v2u*)(img + line * 64 + (((2 * cq + 1) ^ (e & 7)) * 8));
                v4u v = {x0.x, x0.y, x1.x, x1.y};
                const int seg = rb * 4 + (line >> 7);
                __builtin_amdgcn_raw_buffer_store_b128(v, r, (unsigned)((((seg * NH + head) * DH + e) * 32 + cq * 8) * 2), 0, 16);
            }
        } else {
            const __amdgpu_buffer_rsrc_t r = __builtin_amdgcn_make_buffer_rsrc(mat == 0 ? a.K : a.Q, 0, S_ * HD * 2, 0x00020000);
#pragma unroll
            for (int i = 0; i < 8; ++i) {
                const int q = tid + 256 * i;
                const int row = q >> 4, ch = q & 15;
                v4u v = *(const v4u*)(img + row * 256 + ((ch ^ (row & 15)) * 16));
                __builtin_amdgcn_raw_buffer_store_b128(v, r, (unsigned)(((rb * 128 + row) * HD + head * DH + ch * 8) * 2), 0, 16);
            }
        }
        if (rb == 0 || rb == 15) {
            asm volatile("s_waitcnt vmcnt(0) lgkmcnt(0)" ::: "memory");
            __builtin_amdgcn_s_barrier();
            if (tid == 0) __hip_atomic_store(a.flags + (rb * 96 + c) * FLAG_STRIDE, 1u, __ATOMIC_RELAXED, __HIP_MEMORY_SCOPE_AGENT);
        }
    }
    asm volatile("s_waitcnt vmcnt(0)" ::: "memory");
    __syncthreads();
}

__device__ __forceinline__ void attn_unit(const RecArgs& a, int seg, int h, int lane) {
    const int G = lane >> 4, l15 = lane & 15;
    const float scale = 0.08838834764831845f;
    const __amdgpu_buffer_rsrc_t rq = __builtin_amdgcn_make_buffer_rsrc(a.Q, 0, S_ * HD * 2, 0x00020000);
    const __amdgpu_buffer_rsrc_t rk = __builtin_amdgcn_make_buffer_rsrc(a.K, 0, S_ * HD * 2, 0x00020000);
    const __amdgpu_buffer_rsrc_t rv = __builtin_amdgcn_make_buffer_rsrc(a.VT, 0, S_ * HD * 2, 0x00020000);
    const __amdgpu_buffer_rsrc_t rkm = __builtin_amdgcn_make_buffer_rsrc(a.KM, 0, NSEG * 8 * HD * 2, 0x00020000);
    const __amdgpu_buffer_rsrc_t rvm = __builtin_amdgcn_make_buffer_rsrc(a.VMT, 0, NSEG * 8 * HD * 2, 0x00020000);
    const int rb = seg >> 2;
    wait_flag(a.flags + (rb * 96 + h) * FLAG_STRIDE);
    wait_flag(a.flags + (rb * 96 + 32 + h) * FLAG_STRIDE);
    wait_flag(a.flags + (rb * 96 + 64 + h) * FLAG_STRIDE);
    v4u k1[4], k2[4], q0[4], q1[4];
    v2u v1[8], v2[8];
#pragma unroll
    for (int ks = 0; ks < 4; ++ks) {
        const unsigned eo = (unsigned)(h * DH + ks * 32 + 8 * G) * 2;
        k1[ks] = __builtin_amdgcn_raw_buffer_load_b128(rk, (unsigned)(seg * 32 + l15) * (HD * 2) + eo, 0, 16);
        k2[ks] = __builtin_amdgcn_raw_buffer_load_b128(rk, (unsigned)(seg * 32 + 16 + l15) * (HD * 2) + eo, 0, 16);
        q0[ks] = __builtin_amdgcn_raw_buffer_load_b128(rq, (unsigned)(seg * 32 + l15) * (HD * 2) + eo, 0, 16);
        q1[ks] = __builtin_amdgcn_raw_buffer_load_b128(rq, (unsigned)(seg * 32 + 16 + l15) * (HD * 2) + eo, 0, 16);
    }
#pragma unroll
    for (int et = 0; et < 8; ++et) {
        const unsigned vo = (unsigned)(((seg * NH + h) * DH + et * 16 + l15) * 32 + 4 * G) * 2;
        v1[et] = __builtin_amdgcn_raw_buffer_load_b64(rv, vo, 0, 16);
        v2[et] = __builtin_amdgcn_raw_buffer_load_b64(rv, vo + 32, 0, 16);
    }
    v4u k0[4]; v2u vm[8];
    {
        unsigned spins = 0;
        const unsigned* probe = (const unsigned*)(a.VMT + ((size_t)(seg * NH + h) * DH + 127) * 8);
        while (__builtin_amdgcn_readfirstlane(__hip_atomic_load(probe, __ATOMIC_RELAXED, __HIP_MEMORY_SCOPE_AGENT)) == 0xFFFFFFFFu) {
            if (seg >= NSEG - 2) __builtin_amdgcn_s_sleep(10); else __builtin_amdgcn_s_sleep(100);
            if (++spins > SPIN_CAP) break;
        }
        for (;;) {
            unsigned mx = 0;
#pragma unroll
            for (int ks = 0; ks < 4; ++ks) {
                k0[ks] = __builtin_amdgcn_raw_buffer_load_b128(rkm, (unsigned)((seg * 8 + (l15 & 7)) * HD + h * DH + ks * 32 + 8 * G) * 2, 0, 16);
            }
#pragma unroll
            for (int et = 0; et < 8; ++et) vm[et] = __builtin_amdgcn_raw_buffer_load_b64(rvm, (unsigned)(((seg * NH + h) * DH + et * 16 + l15) * 8 + 4 * (G & 1)) * 2, 0, 16);
#pragma unroll
            for (int ks = 0; ks < 4; ++ks) mx = max(mx, umax4(k0[ks]));
#pragma unroll
            for (int et = 0; et < 8; ++et) mx = umax3(mx, vm[et].x, vm[et].y);
            asm volatile("" ::: "memory");
            if (__all(mx != 0xFFFFFFFFu)) break;
            __builtin_amdgcn_s_sleep(16);
            if (++spins > SPIN_CAP) break;
        }
    }
    f32x4 st[3][2];
#pragma unroll
    for (int i = 0; i < 3; ++i) { st[i][0] = (f32x4){0.f, 0.f, 0.f, 0.f}; st[i][1] = st[i][0]; }
#pragma unroll
    for (int ks = 0; ks < 4; ++ks) {
        st[0][0] = mfma16(as8(k0[ks]), as8(q0[ks]), st[0][0]); st[0][1] = mfma16(as8(k0[ks]), as8(q1[ks]), st[0][1]);
        st[1][0] = mfma16(as8(k1[ks]), as8(q0[ks]), st[1][0]); st[1][1] = mfma16(as8(k1[ks]), as8(q1[ks]), st[1][1]);
        st[2][0] = mfma16(as8(k2[ks]), as8(q0[ks]), st[2][0]); st[2][1] = mfma16(as8(k2[ks]), as8(q1[ks]), st[2][1]);
    }
    bf16x8 pf[2][2];
#pragma unroll
    for (int qt = 0; qt < 2; ++qt) {
        const int t = qt * 16 + l15;
        float v[3][4];
        float m = -3.0e38f;
#pragma unroll
        for (int i = 0; i < 3; ++i)
#pragma unroll
            for (int r = 0; r < 4; ++r) {
                float sc = bfr(bfr(st[i][qt][r]) * scale);
                bool ok;
                if (i == 0) ok = (G < 2); else ok = ((i - 1) * 16 + 4 * G + r) <= t;
                bool exist = (i != 0) || (G < 2);
                sc = ok ? sc : bfr(-1.0e9f);
                v[i][r] = exist ? sc : -3.0e38f;
                m = fmaxf(m, v[i][r]);
            }
        m = xrow16_max(m);
        float sum = 0.f;
#pragma unroll
        for (int i = 0; i < 3; ++i)
#pragma unroll
            for (int r = 0; r < 4; ++r) { bool exist = (i != 0) || (G < 2); v[i][r] = exist ? __expf(v[i][r] - m) : 0.f; sum += v[i][r]; }
        sum = xrow16_sum(sum);
        const float inv = 1.0f / sum;
        v4u p0 = {pack2(v[0][0] * inv, v[0][1] * inv), pack2(v[0][2] * inv, v[0][3] * inv), pack2(v[1][0] * inv, v[1][1] * inv), pack2(v[1][2] * inv, v[1][3] * inv)};
        v4u p1 = {pack2(v[2][0] * inv, v[2][1] * inv), pack2(v[2][2] * inv, v[2][3] * inv), 0u, 0u};
        pf[qt][0] = as8(p0); pf[qt][1] = as8(p1);
    }
#pragma unroll
    for (int et = 0; et < 8; ++et) {
        bf16x8 a0 = mk8(vm[et], v1[et]);
        bf16x8 a1 = mk8(v2[et], (v2u){0u, 0u});
#pragma unroll
        for (int qt = 0; qt < 2; ++qt) {
            f32x4 o = (f32x4){0.f, 0.f, 0.f, 0.f};
            o = mfma16(a0, pf[qt][0], o);
            o = mfma16(a1, pf[qt][1], o);
            v2u ov = {pack2(o[0], o[1]), pack2(o[2], o[3])};
            __hip_atomic_store((unsigned long long*)(a.O + ((size_t)seg * 32 + qt * 16 + l15) * HD + h * DH + et * 16 + 4 * G), __builtin_bit_cast(unsigned long long, ov), __ATOMIC_RELAXED, __HIP_MEMORY_SCOPE_AGENT);
        }
    }
}

constexpr int L_WO = 0;
constexpr int L_MEM = 65536;
constexpr int L_T = L_MEM + 8192;
constexpr int L_VM = L_T + 4096;
constexpr int L_STG = 96 * 1024;
constexpr int L_TOTAL = 160 * 1024;

__global__ void __launch_bounds__(256, 1) k_rec(RecArgs a) {
    extern __shared__ __attribute__((aligned(16))) unsigned char lds[];
    const int tid = threadIdx.x, lane = tid & 63;
    const int wave = __builtin_amdgcn_readfirstlane(tid >> 6);
    const int G = lane >> 4, l15 = lane & 15;
    const __amdgpu_buffer_rsrc_t rmem = __builtin_amdgcn_make_buffer_rsrc(a.memx, 0, (NSEG + 1) * MEMX_STEP, 0x00020000);
    const __amdgpu_buffer_rsrc_t rpart = __builtin_amdgcn_make_buffer_rsrc(a.part, 0, 2 * PART_BUF, 0x00020000);

    const int bid = blockIdx.x;
    if (bid >= 2 * NX) {
        const int w = bid - 2 * NX;
        if (w >= 32) {
            const int c = w - 32;
            if ((c >> 5) == 1) proj_col<true>(a, lds, c); else proj_col<false>(a, lds, c);
        }
        if (w < 32) {
            for (int rb = 0; rb < 16; ++rb) {
                attn_unit(a, rb * 4 + (w >> 3), (w & 7) * 4 + wave, lane);
                signal_count(a.flags + (CNT_BASE + rb * 4) * FLAG_STRIDE);
            }
        } else if (w < 80) {
            const int t = (w - 32) & 15;
            for (int rb = (w - 32) >> 4; rb < RB_W; rb += 3) {
                unsigned* cnt = a.flags + (CNT_BASE + rb * 4) * FLAG_STRIDE;
                wait_count_acquire(cnt, 32);
                GemmArgs g{a.O, a.Wo, nullptr, nullptr, a.x, a.hb, nullptr, nullptr, HD, DM, HD, 0};
                gemm_body<3, 64, 1, 8, true>(g, lds, rb * 128 + (t >> 3) * 64, (t & 7) * 64, 0);
                signal_count(cnt + FLAG_STRIDE);
                wait_count_acquire(cnt + FLAG_STRIDE, 16);
                const __amdgpu_buffer_rsrc_t rh = __builtin_amdgcn_make_buffer_rsrc(a.hn, 0, S_ * DM * 2, 0x00020000);
                const int row0 = rb * 128 + t * 8 + wave * 2;
                v4u h0 = *(const v4u*)(a.hb + (size_t)row0 * DM + lane * 8), h1 = *(const v4u*)(a.hb + (size_t)(row0 + 1) * DM + lane * 8);
                __builtin_amdgcn_raw_buffer_store_b128(layernorm_vals(h0), rh, (unsigned)(row0 * DM + lane * 8) * 2, 0, 16);
                __builtin_amdgcn_raw_buffer_store_b128(layernorm_vals(h1), rh, (unsigned)((row0 + 1) * DM + lane * 8) * 2, 0, 16);
                signal_count(cnt + 2 * FLAG_STRIDE);
            }
        } else if (w < 80 + 32) {
            for (int u = w - 80; u < RB_F1 * 28; u += 32) {
                const int rb = u / 28;
                unsigned* cnt = a.flags + (CNT_BASE + rb * 4) * FLAG_STRIDE;
                wait_count_acquire(cnt + 2 * FLAG_STRIDE, 16);
                GemmArgs g{a.hn, a.Wg, a.Wu, nullptr, nullptr, a.act, nullptr, nullptr, DM, FF, DM, 0};
                gemm_body<2, 128, 2, 4, true>(g, lds, rb * 128, (u % 28) * 64, 0);
                signal_count(cnt + 3 * FLAG_STRIDE);
            }
        } else {
            for (int rb = (w - 80 - 32) >> 3; rb < RB_IN; rb += (48 - 32) / 8) {
                unsigned* cnt = a.flags + (CNT_BASE + rb * 4) * FLAG_STRIDE;
                wait_count_acquire(cnt + 3 * FLAG_STRIDE, 28);
                GemmArgs g{a.act, a.Wd, nullptr, nullptr, a.hb, a.out, nullptr, nullptr, FF, DM, FF, 0};
                gemm_body<3, 128, 1, 6, false>(g, lds, rb * 128, (w & 7) * 64, 0);
                __syncthreads();
            }
        }
        return;
    }
    if (bid >= NX) {
        if (wave != 0) return;
        const int y = bid - NX;
        bf16x8 sel;
#pragma unroll
        for (int j = 0; j < 8; ++j) sel[j] = (l15 == j) ? (__bf16)1.0f : (__bf16)0.0f;
        for (int s = 0; s < NSEG - 1; ++s) {
            const unsigned base = (s & 1) * PART_BUF + y * 8192 + lane * 16;
            v4u m[8];
            unsigned spins = 0;
            {
                unsigned sp2 = 0;
                while (__builtin_amdgcn_readfirstlane(__hip_atomic_load(a.flags + (SIG_BASE + 2 * y) * FLAG_STRIDE, __ATOMIC_RELAXED, __HIP_MEMORY_SCOPE_AGENT)) < (unsigned)(s + 1)) {
                    if (++sp2 > SPIN_CAP) break;
                }
                __builtin_amdgcn_s_sleep(6);
                asm volatile("" ::: "memory");
            }
            f32x4 acc0, acc1;
            for (;;) {
#pragma unroll
                for (int t = 0; t < 8; ++t) m[t] = __builtin_amdgcn_raw_buffer_load_b128(rpart, base + t * 1024, 0, 16);
                unsigned mx = 0;
                acc0 = (f32x4){0.f, 0.f, 0.f, 0.f}; acc1 = acc0;
#pragma unroll
                for (int t = 0; t < 8; t += 2) {
                    acc0 = mfma16(sel, as8(m[t]), acc0); acc1 = mfma16(sel, as8(m[t + 1]), acc1);
                    mx = umax3(mx, umax3(m[t].x, m[t].y, m[t].z), m[t].w);
                    mx = umax3(mx, umax3(m[t + 1].x, m[t + 1].y, m[t + 1].z), m[t + 1].w);
                }
                asm volatile("" : "+v"(acc0), "+v"(acc1) :: "memory");
                if (__all(mx != 0xFFFFFFFFu)) break;
                if (++spins > SPIN_CAP) break;
            }
            if (lane == 0) __hip_atomic_store(a.flags + (SIG_BASE + 128 + 2 * y) * FLAG_STRIDE, (unsigned)(s + 1), __ATOMIC_RELAXED, __HIP_MEMORY_SCOPE_AGENT);
            acc0 += acc1;
#pragma unroll
            for (int r = 0; r < 4; ++r) acc0[r] += dppf<0x128>(acc0[r]);
            if (lane < 32 && l15 < 8) {
                v2u v = {nosent(pack2(acc0[0], acc0[1])), nosent(pack2(acc0[2], acc0[3]))};
                __builtin_amdgcn_raw_buffer_store_b64(v, rmem, (s + 1) * MEMX_STEP + y * 128 + l15 * 16 + G * 8, 0, 16);
            }
            const v4u sent = {0xFFFFFFFFu, 0xFFFFFFFFu, 0xFFFFFFFFu, 0xFFFFFFFFu};
#pragma unroll
            for (int t = 0; t < 8; ++t) __builtin_amdgcn_raw_buffer_store_b128(sent, rpart, base + t * 1024, 0, 16);
            asm volatile("s_waitcnt vmcnt(0)" ::: "memory");
        }
        return;
    }

    const int b = bid, h = b >> 1, g = b & 1;
    bf16x16 wqk[2][16];
    bf16x8 wv[16];
    {
        const int tq_ = l15 >> 2, tp_ = lane & 3;
        const unsigned stg = __builtin_amdgcn_readfirstlane(lds_addr(lds)) + wave * 1024;
        int srow[4], sch[4];
#pragma unroll
        for (int i = 0; i < 4; ++i) { const int q = tid + 256 * i; srow[i] = q >> 4; sch[i] = ((q & 15) ^ (((srow[i] & 3) << 2) | ((srow[i] >> 2) & 3))) * 8; }
        auto issue = [&](int c) {
            const u16* s0; const u16* s1; int ld;
            if (c < 8) { s0 = a.Wq + (size_t)(64 * c) * HD + h * DH; s1 = a.Wk + (size_t)(64 * c) * HD + h * DH; ld = HD; }
            else if (c < 12) { s0 = a.Wv + (size_t)(128 * (c - 8)) * HD + h * DH; s1 = s0 + (size_t)64 * HD; ld = HD; }
            else { s0 = a.Wo + (size_t)(h * DH + 64 * g) * DM + 256 * (c - 12); s1 = s0 + 128; ld = DM; }
            const unsigned dst = stg + (((c & 3) < 2) ? (unsigned)(L_STG + (c & 1) * 32768) : (unsigned)(L_WO + (c & 1) * 32768));
#pragma unroll
            for (int i = 0; i < 4; ++i) glds16(s0 + (size_t)srow[i] * ld + sch[i], dst + i * 4096);
#pragma unroll
            for (int i = 0; i < 4; ++i) glds16(s1 + (size_t)srow[i] * ld + sch[i], dst + 16384 + i * 4096);
        };
        issue(0); issue(1); issue(2);
#pragma unroll
        for (int c = 0; c < 14; ++c) {
            if (c + 3 < 14) { issue(c + 3); asm volatile("s_waitcnt vmcnt(24)" ::: "memory"); }
            else if (c + 3 == 14) asm volatile("s_waitcnt vmcnt(16)" ::: "memory");
            else if (c + 2 == 14) asm volatile("s_waitcnt vmcnt(8)" ::: "memory");
            else asm volatile("s_waitcnt vmcnt(0)" ::: "memory");
            __builtin_amdgcn_s_barrier();
            const unsigned char* t0 = lds + (((c & 3) < 2) ? (L_STG + (c & 1) * 32768) : (L_WO + (c & 1) * 32768));
            const unsigned char* t1 = t0 + 16384;
            if (c < 8) {
#pragma unroll
                for (int tt = 0; tt < 2; ++tt)
#pragma unroll
                    for (int u = 0; u < 2; ++u) {
                        v4u hf[2];
#pragma unroll
                        for (int half = 0; half < 2; ++half) {
                            const unsigned off = offB(32 * tt + 16 * half + 4 * G + tq_, 2 * (2 * wave + u) + (tp_ >> 1)) + 8 * (tp_ & 1);
                            v2u q = tr_read(t0 + off), k = tr_read(t1 + off);
                            hf[half] = (v4u){q.x, k.x, q.y, k.y};
                        }
                        bf16x8 lo = as8(hf[0]), hi = as8(hf[1]);
#pragma unroll
                        for (int j = 0; j < 8; ++j) { wqk[u][2 * c + tt][j] = lo[j]; wqk[u][2 * c + tt][8 + j] = hi[j]; }
                    }
            } else if (c < 12) {
#pragma unroll
                for (int j = 0; j < 2; ++j)
#pragma unroll
                    for (int tt = 0; tt < 2; ++tt) {
                        const unsigned char* tb = j ? t1 : t0;
                        v2u lo = tr_read(tb + offB(32 * tt + 8 * G + tq_, 2 * (4 * g + wave) + (tp_ >> 1)) + 8 * (tp_ & 1));
                        v2u hi = tr_read(tb + offB(32 * tt + 8 * G + 4 + tq_, 2 * (4 * g + wave) + (tp_ >> 1)) + 8 * (tp_ & 1));
                        wv[4 * (c - 8) + 2 * j + tt] = mk8(lo, hi);
                    }
            } else {
#pragma unroll
                for (int j = 0; j < 2; ++j)
#pragma unroll
                    for (int kk = 0; kk < 2; ++kk)
#pragma unroll
                        for (int half = 0; half < 2; ++half) {
                            const unsigned char* tb = j ? t1 : t0;
                            const int e0 = 32 * kk + 16 * (G & 1) + 4 * (2 * half + (G >> 1)) + tq_;
                            v2u A = tr_read(tb + offB(e0, 4 * wave + (tp_ >> 1)) + 8 * (tp_ & 1));
                            v2u B = tr_read(tb + offB(e0, 4 * wave + 2 + (tp_ >> 1)) + 8 * (tp_ & 1));
                            const int dp = 8 * (c - 12) + 4 * j + wave;
                            *(v4u*)(lds + L_WO + ((dp * 2 + kk) * 2 + half) * 1024 + lane * 16) = (v4u){A.x, B.x, A.y, B.y};
                        }
            }
            asm volatile("s_waitcnt lgkmcnt(0)" ::: "memory");
            __builtin_amdgcn_s_barrier();
        }
    }
    const int sidx = (lane & 8) ? 0xEEEE : 0x4444;
    const int tq = l15 >> 2, tp = lane & 3;
    const unsigned char* memrd = lds + L_MEM + (8 * G + tq) * 16 + (tp & 1) * 8;
    const unsigned char* trd = lds + L_T + (8 * G + tq) * 32 + (tp & 1) * 8;
    const unsigned char* wp = lds + L_WO + wave * 16384 + lane * 16;
    const float sl2e = 0.08838834764831845f * 1.4426950408889634f;
    const bool exist0 = (G < 2);
    constexpr int PD = 4;
    const __amdgpu_buffer_rsrc_t rk = __builtin_amdgcn_make_buffer_rsrc(a.K, 0, S_ * HD * 2, 0x00020000);
    const __amdgpu_buffer_rsrc_t rv = __builtin_amdgcn_make_buffer_rsrc(a.VT, 0, S_ * HD * 2, 0x00020000);
    const __amdgpu_buffer_rsrc_t rkm = __builtin_amdgcn_make_buffer_rsrc(a.KM, 0, NSEG * 8 * HD * 2, 0x00020000);
    const __amdgpu_buffer_rsrc_t rvm = __builtin_amdgcn_make_buffer_rsrc(a.VMT, 0, NSEG * 8 * HD * 2, 0x00020000);

    wait_flag(a.flags + h * FLAG_STRIDE);
    wait_flag(a.flags + (32 + h) * FLAG_STRIDE);
    v4u kx[2][4]; v2u vx[2];
    auto load_kx = [&](int s) {
#pragma unroll
        for (int kt = 0; kt < 2; ++kt)
#pragma unroll
            for (int ks = 0; ks < 4; ++ks) kx[kt][ks] = __builtin_amdgcn_raw_buffer_load_b128(rk, (unsigned)((s * 32 + kt * 16 + l15) * HD + h * DH + ks * 32 + 8 * G) * 2, 0, 16);
        const unsigned vo = (unsigned)(((s * NH + h) * DH + 64 * g + wave * 16 + l15) * 32 + 4 * G) * 2;
        vx[0] = __builtin_amdgcn_raw_buffer_load_b64(rv, vo, 0, 16); vx[1] = __builtin_amdgcn_raw_buffer_load_b64(rv, vo + 32, 0, 16);
    };
    load_kx(0);
    for (int s = 0; s < NSEG; ++s) {
        const int sn = (s + 1 < NSEG) ? s + 1 : s;
        {
            v4u m0, m1;
            unsigned spins = 0;
            {
                unsigned sp2 = 0;
                while (__builtin_amdgcn_readfirstlane(__hip_atomic_load(a.flags + (SIG_BASE + 128 + 2 * b) * FLAG_STRIDE, __ATOMIC_RELAXED, __HIP_MEMORY_SCOPE_AGENT)) < (unsigned)s) {
                    if (++sp2 > SPIN_CAP) break;
                }
                asm volatile("" ::: "memory");
            }
            for (;;) {
                m0 = __builtin_amdgcn_raw_buffer_load_b128(rmem, s * MEMX_STEP + tid * 16, 0, 16);
                m1 = __builtin_amdgcn_raw_buffer_load_b128(rmem, s * MEMX_STEP + 4096 + tid * 16, 0, 16);
                *(v4u*)(lds + L_MEM + tid * 16) = m0;
                *(v4u*)(lds + L_MEM + 4096 + tid * 16) = m1;
                unsigned mx = umax3(umax3(m0.x, m0.y, m0.z), umax3(m0.w, m1.x, m1.y), max(m1.z, m1.w));
                asm volatile("" ::: "memory");
                if (__all(mx != 0xFFFFFFFFu)) break;
                if (++spins > SPIN_CAP) break;
            }
        }
        f32x4 acc0 = (f32x4){0.f, 0.f, 0.f, 0.f}, acc1 = acc0;
        asm volatile("" : "+v"(acc0), "+v"(acc1));
        __syncthreads();
        v2u lo[16], hi[16];
#pragma unroll
        for (int t = 0; t < PD; ++t) { lo[t] = tr_read(memrd + t * 512); hi[t] = tr_read(memrd + t * 512 + 64); }
        SB0();
#pragma unroll
        for (int t = 0; t < 16; ++t) {
            if (t + PD < 16) { lo[t + PD] = tr_read(memrd + (t + PD) * 512); hi[t + PD] = tr_read(memrd + (t + PD) * 512 + 64); }
            acc0 = __builtin_amdgcn_smfmac_f32_16x16x64_bf16(mk8(lo[t], hi[t]), wqk[0][t], acc0, sidx, 0, 0);
            acc1 = __builtin_amdgcn_smfmac_f32_16x16x64_bf16(mk8(lo[t], hi[t]), wqk[1][t], acc1, sidx, 0, 0);
            SB0();
        }
        {
            v2u v0 = {pack2(acc0[0], acc0[1]), pack2(acc0[2], acc0[3])};
            v2u v1 = {pack2(acc1[0], acc1[1]), pack2(acc1[2], acc1[3])};
            *(v2u*)(lds + L_T + (wave * 32 + l15) * 32 + G * 8) = v0;
            *(v2u*)(lds + L_T + (wave * 32 + 16 + l15) * 32 + G * 8) = v1;
        }
        __syncthreads();
        v2u q_lo[4], q_hi[4], k_lo[4], k_hi[4];
#pragma unroll
        for (int ks = 0; ks < 4; ++ks) {
            q_lo[ks] = tr_read(trd + ks * 1024); q_hi[ks] = tr_read(trd + ks * 1024 + 128);
            k_lo[ks] = tr_read(trd + ks * 1024 + 16); k_hi[ks] = tr_read(trd + ks * 1024 + 128 + 16);
        }
        f32x4 accv = (f32x4){0.f, 0.f, 0.f, 0.f};
#pragma unroll
        for (int t = 0; t < 4; ++t) accv = mfma16(mk8(lo[t], hi[t]), wv[t], accv);
        SB0();
        f32x4 st[3];
#pragma unroll
        for (int i = 0; i < 3; ++i) st[i] = (f32x4){0.f, 0.f, 0.f, 0.f};
#pragma unroll
        for (int ks = 0; ks < 4; ++ks) {
            bf16x8 qf = mk8(q_lo[ks], q_hi[ks]);
            st[1] = mfma16(as8(kx[0][ks]), qf, st[1]);
            st[2] = mfma16(as8(kx[1][ks]), qf, st[2]);
            st[0] = mfma16(mk8(k_lo[ks], k_hi[ks]), qf, st[0]);
        }
        v4u wo[4][2][2];
#pragma unroll
        for (int i = 0; i < 4; ++i) { wo[i][0][0] = *(const v4u*)(wp + i * 4096); wo[i][0][1] = *(const v4u*)(wp + i * 4096 + 1024); }
        SB0();
#pragma unroll
        for (int t = 4; t < 16; ++t) accv = mfma16(mk8(lo[t], hi[t]), wv[t], accv);
        float v[3][4];
#pragma unroll
        for (int i = 0; i < 3; ++i)
#pragma unroll
            for (int r = 0; r < 4; ++r) { v[i][r] = st[i][r] * sl2e; asm("" : "+v"(v[i][r])); }
        float m = fmaxf(fmaxf(fmaxf(v[1][0], v[1][1]), fmaxf(v[1][2], v[1][3])), fmaxf(fmaxf(v[2][0], v[2][1]), fmaxf(v[2][2], v[2][3])));
        float m0 = fmaxf(fmaxf(v[0][0], v[0][1]), fmaxf(v[0][2], v[0][3]));
        m = exist0 ? fmaxf(m, m0) : m;
        m = xrow16_max(m);
#pragma unroll
        for (int i = 0; i < 3; ++i)
#pragma unroll
            for (int r = 0; r < 4; ++r) v[i][r] = __builtin_amdgcn_exp2f(v[i][r] - m);
#pragma unroll
        for (int r = 0; r < 4; ++r) v[0][r] = exist0 ? v[0][r] : 0.f;
        v4u p0 = {pack2(v[0][0], v[0][1]), pack2(v[0][2], v[0][3]), pack2(v[1][0], v[1][1]), pack2(v[1][2], v[1][3])};
        v4u p1 = {pack2(v[2][0], v[2][1]), pack2(v[2][2], v[2][3]), 0u, 0u};
        v2u vm = {pack2(accv[0], accv[1]), pack2(accv[2], accv[3])};
        f32x4 o = mfma16(mk8(vx[1], (v2u){0u, 0u}), as8(p1), (f32x4){0.f, 0.f, 0.f, 0.f});
        o = mfma16(mk8(vm, vx[0]), as8(p0), o);
        float sum = ((v[0][0] + v[0][1]) + (v[0][2] + v[0][3])) + ((v[1][0] + v[1][1]) + (v[1][2] + v[1][3])) + ((v[2][0] + v[2][1]) + (v[2][2] + v[2][3]));
        sum = xrow16_sum(sum);
        const float inv = __builtin_amdgcn_rcpf(sum);
        *(v2u*)(lds + L_VM + wave * 512 + lane * 8) = (v2u){pack2(o[0] * inv, o[1] * inv), pack2(o[2] * inv, o[3] * inv)};
        f32x4 d[4];
#pragma unroll
        for (int i = 0; i < 4; ++i) d[i] = (f32x4){0.f, 0.f, 0.f, 0.f};
        asm volatile("" : "+v"(d[0]), "+v"(d[1]), "+v"(d[2]), "+v"(d[3]));
        __syncthreads();
        if (tid == 0) __hip_atomic_store(a.flags + (SIG_BASE + 2 * b) * FLAG_STRIDE, (unsigned)(s + 1), __ATOMIC_RELAXED, __HIP_MEMORY_SCOPE_AGENT);
        v2u ob[4];
#pragma unroll
        for (int et = 0; et < 4; ++et) ob[et] = *(const v2u*)(lds + L_VM + et * 512 + lane * 8);
#pragma unroll
        for (int i = 0; i < 4; ++i) { wo[i][1][0] = *(const v4u*)(wp + i * 4096 + 2048); wo[i][1][1] = *(const v4u*)(wp + i * 4096 + 3072); }
        SB0();
        const bf16x8 oa0 = mk8(ob[0], ob[1]), oa1 = mk8(ob[2], ob[3]);
#pragma unroll
        for (int kk = 0; kk < 2; ++kk)
#pragma unroll
            for (int i = 0; i < 4; ++i) {
                bf16x16 wf;
                bf16x8 w0b = as8(wo[i][kk][0]), w1b = as8(wo[i][kk][1]);
#pragma unroll
                for (int j = 0; j < 8; ++j) { wf[j] = w0b[j]; wf[8 + j] = w1b[j]; }
                d[i] = __builtin_amdgcn_smfmac_f32_16x16x64_bf16(kk == 0 ? oa0 : oa1, wf, d[i], sidx, 0, 0);
            }
#pragma unroll
        for (int i = 0; i < 4; ++i) {
            const int dcol = 32 * (wave * 4 + i) + 16 * (G >> 1) + l15;
            v2u pv = {nosent(pack2(d[i][0], d[i][1])), nosent(pack2(d[i][2], d[i][3]))};
            __builtin_amdgcn_raw_buffer_store_b64(pv, rpart, (s & 1) * PART_BUF + (dcol >> 3) * 8192 + b * 128 + (dcol & 7) * 16 + (G & 1) * 8, 0, 16);
        }
        if (G < 2) {
            v2u vs = {nosent(vm.x), nosent(vm.y)};
            __builtin_amdgcn_raw_buffer_store_b64(vs, rvm, (unsigned)(((s * NH + h) * DH + 64 * g + wave * 16 + l15) * 8 + 4 * G) * 2, 0, 16);
        }
        if (g == 0 && l15 < 8) {
            v2u kl = wave == 0 ? k_lo[0] : (wave == 1 ? k_lo[1] : (wave == 2 ? k_lo[2] : k_lo[3]));
            v2u kh = wave == 0 ? k_hi[0] : (wave == 1 ? k_hi[1] : (wave == 2 ? k_hi[2] : k_hi[3]));
            v4u kv = {nosent(kl.x), nosent(kl.y), nosent(kh.x), nosent(kh.y)};
            __builtin_amdgcn_raw_buffer_store_b128(kv, rkm, (unsigned)((s * 8 + l15) * HD + h * DH + wave * 32 + 8 * G) * 2, 0, 16);
        }
        SB0();
        load_kx(sn);
        if ((s & 3) == 2 && s + 2 < NSEG) {
            wait_flag(a.flags + (((s + 2) >> 2) * 96 + h) * FLAG_STRIDE);
            wait_flag(a.flags + (((s + 2) >> 2) * 96 + 32 + h) * FLAG_STRIDE);
        }
    }
}

extern "C" void kernel_launch(void* const* d_in, const int* in_sizes, int n_in, void* d_out, int out_size, void* d_ws, size_t ws_size,
                              hipStream_t stream) {
    const u16* x = (const u16*)d_in[0];
    const u16* mem0 = (const u16*)d_in[1];
    const u16* Wq = (const u16*)d_in[2];
    const u16* Wk = (const u16*)d_in[3];
    const u16* Wv = (const u16*)d_in[4];
    const u16* Wo = (const u16*)d_in[5];
    const u16* Wg = (const u16*)d_in[8];
    const u16* Wu = (const u16*)d_in[9];
    const u16* Wd = (const u16*)d_in[10];
    u16* out = (u16*)d_out;

    unsigned char* ws = (unsigned char*)d_ws;
    size_t off = 0;
    auto alloc = [&](size_t bytes) { unsigned char* p = ws + off; off += (bytes + 255) & ~(size_t)255; return p; };
    constexpr size_t KM_BYTES = (size_t)NSEG * 8 * HD * 2;
    constexpr size_t SENT_BYTES = (size_t)2 * PART_BUF + (size_t)(NSEG + 1) * MEMX_STEP + 2 * KM_BYTES;
    unsigned* flags = (unsigned*)alloc((16 * 96 + 64 + 256) * FLAG_STRIDE * 4);
    unsigned char* xchg = alloc(SENT_BYTES);
    unsigned char* part = xchg;
    unsigned char* memx = xchg + 2 * PART_BUF;
    u16* KM = (u16*)(memx + (size_t)(NSEG + 1) * MEMX_STEP);
    u16* VMT = (u16*)((unsigned char*)KM + KM_BYTES);
    u16* xn = (u16*)alloc((size_t)S_ * DM * 2);
    u16* Q = (u16*)alloc((size_t)S_ * HD * 2);
    u16* K = (u16*)alloc((size_t)S_ * HD * 2);
    u16* VT = (u16*)alloc((size_t)S_ * HD * 2);
    u16* O = (u16*)alloc((size_t)S_ * HD * 2);
    u16* hb = (u16*)alloc((size_t)S_ * DM * 2);
    u16* hn = (u16*)alloc((size_t)S_ * DM * 2);
    u16* act = (u16*)alloc((size_t)S_ * FF * 2);
    float* Pk = (float*)alloc((size_t)16 * S_ * DM * 4);

    k_prep<<<S_ / 4, 256, 0, stream>>>(x, xn, mem0, (u16*)memx, (v4u*)xchg, (int)(SENT_BYTES / 16), (int)(2 * PART_BUF / 16), (v4u*)flags, (16 * 96 + 64 + 256) * FLAG_STRIDE / 4);
    {
        (void)hipFuncSetAttribute((const void*)k_rec, hipFuncAttributeMaxDynamicSharedMemorySize, L_TOTAL);
        RecArgs r{Wo, K, VT, Q, O, KM, VMT, memx, part, xn, Wq, Wk, Wv, flags, x, Wg, Wu, Wd, hb, hn, act, out};
        k_rec<<<2 * NX + NWORK, 256, L_TOTAL, stream>>>(r);
    }
    {
        constexpr int ROW0 = RB_IN * 128, MR = S_ - ROW0;
        constexpr int ROWW = RB_W * 128, MW = S_ - ROWW, ROWF = RB_F1 * 128, MF = S_ - ROWF;
        constexpr int NZ = 8;
        (void)hipFuncSetAttribute((const void*)k_gemm2<1, 128, 1, 5>, hipFuncAttributeMaxDynamicSharedMemorySize, 5 * (128 * 128 + 8192));
        (void)hipFuncSetAttribute((const void*)k_gemm2<2, 64, 2, 4>, hipFuncAttributeMaxDynamicSharedMemorySize, 4 * (64 * 128 + 16384));
        (void)hipFuncSetAttribute((const void*)k_gemm2<3, 32, 1, 8>, hipFuncAttributeMaxDynamicSharedMemorySize, 8 * (32 * 128 + 8192));
        GemmArgs g1{O, Wo, nullptr, nullptr, nullptr, (u16*)Pk, nullptr, nullptr, HD, DM, HD / NZ, (DM / 64) | (NZ << 16)};
        k_gemm2<1, 128, 1, 5><<<(DM / 64) * (MW / 128) * NZ, 256, 5 * (128 * 128 + 8192), stream>>>(g1, ROWW);
        k_res_ln<NZ><<<MW / 4, 256, 0, stream>>>(Pk, x, hb, hn, ROWW);
        GemmArgs g2{hn, Wg, Wu, nullptr, nullptr, act, nullptr, nullptr, DM, FF, DM, (FF / 64) | (1 << 16)};
        k_gemm2<2, 64, 2, 4><<<(FF / 64) * (MF / 64), 256, 4 * (64 * 128 + 16384), stream>>>(g2, ROWF);
        GemmArgs g3{act, Wd, nullptr, nullptr, hb, out, nullptr, nullptr, FF, DM, FF, (DM / 64) | (1 << 16)};
        k_gemm2<3, 32, 1, 8><<<(DM / 64) * (MR / 32), 256, 8 * (32 * 128 + 8192), stream>>>(g3, ROW0);
    }
}
```
